```python
import torch

def _softmax(x, axis=-1):
    return torch.softmax(x, dim=axis)
def _gelu(x):
    return torch.nn.functional.gelu(x, approximate="tanh")
def _silu(x):
    return torch.nn.functional.silu(x)
def _sigmoid(x):
    return torch.sigmoid(x)
def _layernorm(x, axis=-1, eps=1e-5):
    m = x.mean(dim=axis, keepdim=True)
    v = x.var(dim=axis, keepdim=True, unbiased=False)
    return (x - m) / torch.sqrt(v + eps)
def _scatter_add(tgt, idx, src, axis=0):
    return tgt.clone().index_add_(axis, idx, src)
def _segment_sum(src, seg, n):
    out = torch.zeros((n,) + src.shape[1:], dtype=src.dtype)
    return out.index_add_(0, seg, src)
def _pad(x, pw):
    flat = tuple(v for pair in reversed(pw) for v in pair)
    return torch.nn.functional.pad(x, flat)

kernel_name = 'gated_mix__sliding_block_cached__dense_activation_relu__decode'

def setup_inputs():

    _w = (lambda a: (lambda t: t.to(torch.bfloat16) if t.is_floating_point() else t)(torch.as_tensor(a)))
    def _A_setup():
        B, Sq, D = 4, 1, 448
        Hq, Hkv, Sk, Dh = 16, 8, 8192, 256
        assert Hq % Hkv == 0, (Hq, Hkv)
        return {
            "x": _w((lambda *s: torch.randn(*s, dtype=torch.bfloat16) * 0.05)(B, Sq, D)),
            "k_cache": _w((lambda *s: torch.randn(*s, dtype=torch.bfloat16) * 6.4)(B, Hkv, Sk, Dh)),
            "v_cache": _w((lambda *s: torch.randn(*s, dtype=torch.bfloat16) * 0.4)(B, Hkv, Sk, Dh)),
            "wq": _w((lambda *s: torch.randn(*s, dtype=torch.bfloat16) * 6.4)(D, Hq * Dh) * (D ** -0.5)),
            "wo": _w((lambda *s: torch.randn(*s, dtype=torch.bfloat16) * 0.4)(Hq * Dh, D) * ((Hq * Dh) ** -0.5)),
        }
    _A_in = _A_setup()

    def _B_setup():
        B, S, D, Dff = 4, 1, 448, 1792
        return {
            "x": _w((lambda *s: torch.randn(*s, dtype=torch.bfloat16) * 0.05)(B, S, D)),
            "w1": _w((lambda *s: torch.randn(*s, dtype=torch.bfloat16) * 0.05)(D, Dff)),
            "w2": _w((lambda *s: torch.randn(*s, dtype=torch.bfloat16) * 0.05)(Dff, D)),
        }
    _B_in = _B_setup()

    return {
        "A_x": _A_in["x"],
        "A_k_cache": _A_in["k_cache"],
        "A_v_cache": _A_in["v_cache"],
        "A_wq": _A_in["wq"],
        "A_wo": _A_in["wo"],
        "B_x": _B_in["x"],
        "B_w1": _B_in["w1"],
        "B_w2": _B_in["w2"],
        "gate_w": _w((lambda *s: torch.randn(*s, dtype=torch.bfloat16) * 0.05)(448) * 16.0),
    }

def reference(A_x, A_k_cache, A_v_cache, A_wq, A_wo, B_x, B_w1, B_w2, gate_w):
    def _A_ref(x, k_cache, v_cache, wq, wo):
        B, Sq, D = x.shape
        Hq, Dh = 16, 256
        Hkv, Sk = k_cache.shape[1], k_cache.shape[2]
        W = min(4096, max(256, Sk // 4))
        rep = Hq // Hkv
        q = (lambda x, *d: x.permute(*d))((x @ wq).reshape(B, Sq, Hq, Dh), 0, 2, 1, 3)
        k = (lambda x, n, axis: torch.repeat_interleave(x, n, dim=axis))(k_cache[:, :, Sk - W:, :], rep, axis=1)
        v = (lambda x, n, axis: torch.repeat_interleave(x, n, dim=axis))(v_cache[:, :, Sk - W:, :], rep, axis=1)
        scores = torch.einsum("bhqd,bhkd->bhqk", q, k) * (Dh ** -0.5)
        p = _softmax(scores, axis=-1)
        o = torch.einsum("bhqk,bhkd->bhqd", p, v)
        return (lambda x, *d: x.permute(*d))(o, 0, 2, 1, 3).reshape(B, Sq, Hq * Dh) @ wo

    def _B_ref(x, w1, w2):
        h = torch.einsum("bsd,df->bsf", x, w1)
        zero = torch.zeros_like(h)
        h = torch.maximum(h, zero)
        return torch.einsum("bsf,fd->bsd", h, w2)

    x = A_x
    g = _sigmoid((x * gate_w).sum(-1, keepdims=True))
    a = _A_ref(x, A_k_cache, A_v_cache, A_wq, A_wo)
    b = _B_ref(x, B_w1, B_w2)
    return g * a + (1 - g) * b
```

```cpp
#include <hip/hip_runtime.h>
#include <hip/hip_bf16.h>
#include <stdint.h>

typedef unsigned u32x4 __attribute__((ext_vector_type(4)));
typedef unsigned u32x2 __attribute__((ext_vector_type(2)));
typedef float f32x4 __attribute__((ext_vector_type(4)));
typedef __bf16 bf16x2 __attribute__((ext_vector_type(2)));

constexpr int B = 4, D = 448, HQ = 16, HKV = 8, SK = 8192, DH = 256, W = 2048, DFF = 1792;
constexpr int NSPLIT = 8, KPS = W / NSPLIT;

__device__ __forceinline__ float bflo(unsigned u) { return __builtin_bit_cast(float, u << 16); }
__device__ __forceinline__ float bfhi(unsigned u) { return __builtin_bit_cast(float, u & 0xffff0000u); }
__device__ __forceinline__ float bf1(unsigned short u) { return __builtin_bit_cast(float, (unsigned)u << 16); }
__device__ __forceinline__ float rbf(float f) { return (float)(__bf16)f; }
__device__ __forceinline__ unsigned short tobf(float f) { return __builtin_bit_cast(unsigned short, (__bf16)f); }
__device__ __forceinline__ float dot2(unsigned a, unsigned b, float acc) {
  return __builtin_amdgcn_fdot2_f32_bf16(__builtin_bit_cast(bf16x2, a), __builtin_bit_cast(bf16x2, b), acc, false);
}

__global__ void __launch_bounds__(256) k_prep(const unsigned short* x, const unsigned short* wq, const unsigned short* w1,
                                              unsigned short* q, unsigned short* h) {
  __shared__ float xs[B][D];
  __shared__ float red[32][B][64];
  const int tid = threadIdx.x;
  for (int i = tid; i < B * D; i += 256) xs[i / D][i % D] = bf1(x[i]);
  __syncthreads();
  const bool isq = blockIdx.x < 64;
  const unsigned short* w = isq ? wq : w1;
  const int N = isq ? HQ * DH : DFF;
  const int n0 = (isq ? blockIdx.x : blockIdx.x - 64) * 64;
  const int c = tid & 7, r = tid >> 3;
  float acc[B][8] = {};
#pragma unroll
  for (int i = 0; i < 14; ++i) {
    const int k = r + 32 * i;
    u32x4 v = *(const u32x4*)(w + (size_t)k * N + n0 + 8 * c);
#pragma unroll
    for (int b = 0; b < B; ++b) {
      const float xv = xs[b][k];
#pragma unroll
      for (int j = 0; j < 4; ++j) { acc[b][2 * j] += xv * bflo(v[j]); acc[b][2 * j + 1] += xv * bfhi(v[j]); }
    }
  }
#pragma unroll
  for (int b = 0; b < B; ++b)
#pragma unroll
    for (int j = 0; j < 8; ++j) red[r][b][8 * c + j] = acc[b][j];
  __syncthreads();
  const int b = tid >> 6, n = tid & 63;
  float s = 0.f;
  for (int i = 0; i < 32; ++i) s += red[i][b][n];
  if (isq) q[b * N + n0 + n] = tobf(s);
  else h[b * N + n0 + n] = tobf(fmaxf(rbf(s), 0.f));
}

__global__ void __launch_bounds__(512) k_attn(const unsigned short* q, const unsigned short* kc, const unsigned short* vc,
                                              float* po, float* pml) {
  __shared__ float sc[2][KPS];
  __shared__ float red[16][2][DH];
  __shared__ float ml[2][2];
  const int tid = threadIdx.x, lane = tid & 63, wave = tid >> 6;
  const int s = blockIdx.x & 7, kvh = (blockIdx.x >> 3) & 7, b = blockIdx.x >> 6;
  const size_t base = ((size_t)(b * HKV + kvh) * SK + (SK - W) + s * KPS) * DH;
  const int half = lane >> 5, c = lane & 31;
  u32x4 kr[16], vr[16];
#pragma unroll
  for (int i = 0; i < 16; ++i) kr[i] = __builtin_nontemporal_load((const u32x4*)(kc + base + (size_t)(wave * 32 + 2 * i + half) * DH + 8 * c));
#pragma unroll
  for (int i = 0; i < 16; ++i) vr[i] = __builtin_nontemporal_load((const u32x4*)(vc + base + (size_t)(wave * 32 + 2 * i + half) * DH + 8 * c));
  u32x4 q0 = *(const u32x4*)(q + (size_t)b * HQ * DH + (2 * kvh) * DH + 8 * c);
  u32x4 q1 = *(const u32x4*)(q + (size_t)b * HQ * DH + (2 * kvh + 1) * DH + 8 * c);
#pragma unroll
  for (int i = 0; i < 16; ++i) {
    float d0 = 0.f, d1 = 0.f;
#pragma unroll
    for (int j = 0; j < 4; ++j) { d0 = dot2(kr[i][j], q0[j], d0); d1 = dot2(kr[i][j], q1[j], d1); }
#pragma unroll
    for (int o = 16; o >= 1; o >>= 1) { d0 += __shfl_xor(d0, o); d1 += __shfl_xor(d1, o); }
    if (c == 0) { sc[0][wave * 32 + 2 * i + half] = rbf(d0) * 0.0625f; sc[1][wave * 32 + 2 * i + half] = rbf(d1) * 0.0625f; }
  }
  __syncthreads();
  if (wave < 2) {
    float v[4], m = -INFINITY;
#pragma unroll
    for (int j = 0; j < 4; ++j) { v[j] = sc[wave][lane + 64 * j]; m = fmaxf(m, v[j]); }
#pragma unroll
    for (int o = 32; o >= 1; o >>= 1) m = fmaxf(m, __shfl_xor(m, o));
    float l = 0.f;
#pragma unroll
    for (int j = 0; j < 4; ++j) { v[j] = __expf(v[j] - m); l += v[j]; sc[wave][lane + 64 * j] = v[j]; }
#pragma unroll
    for (int o = 32; o >= 1; o >>= 1) l += __shfl_xor(l, o);
    if (lane == 0) { ml[wave][0] = m; ml[wave][1] = l; }
  }
  __syncthreads();
  float a0[8] = {}, a1[8] = {};
#pragma unroll
  for (int i = 0; i < 16; ++i) {
    const float p0 = sc[0][wave * 32 + 2 * i + half], p1 = sc[1][wave * 32 + 2 * i + half];
#pragma unroll
    for (int j = 0; j < 4; ++j) {
      a0[2 * j] += p0 * bflo(vr[i][j]); a0[2 * j + 1] += p0 * bfhi(vr[i][j]);
      a1[2 * j] += p1 * bflo(vr[i][j]); a1[2 * j + 1] += p1 * bfhi(vr[i][j]);
    }
  }
#pragma unroll
  for (int j = 0; j < 8; ++j) { red[wave * 2 + half][0][8 * c + j] = a0[j]; red[wave * 2 + half][1][8 * c + j] = a1[j]; }
  __syncthreads();
  {
    const int hh = tid >> 8, d = tid & 255;
    float t = 0.f;
#pragma unroll
    for (int i = 0; i < 16; ++i) t += red[i][hh][d];
    const int row = b * HQ + 2 * kvh + hh;
    po[((size_t)row * NSPLIT + s) * DH + d] = t;
    if (d < 2) pml[(row * NSPLIT + s) * 2 + d] = ml[hh][d];
  }
}

__global__ void __launch_bounds__(512) k_final(const unsigned short* x, const unsigned short* gw, const unsigned short* h,
                                               const float* po, const float* pml, const unsigned short* wo,
                                               const unsigned short* w2, unsigned short* out) {
  __shared__ float os[B][HQ * DH];
  __shared__ float hs[B][DFF];
  __shared__ float red[8][2][B][8];
  __shared__ float gs[B];
  const int tid = threadIdx.x, lane = tid & 63, wave = tid >> 6;
  const int n0 = blockIdx.x * 8;
  for (int i = tid; i < B * HQ * DH; i += 512) {
    const int row = i >> 8, d = i & 255;
    float M = -INFINITY;
#pragma unroll
    for (int s = 0; s < NSPLIT; ++s) M = fmaxf(M, pml[(row * NSPLIT + s) * 2]);
    float L = 0.f, o = 0.f;
#pragma unroll
    for (int s = 0; s < NSPLIT; ++s) {
      const float wgt = __expf(pml[(row * NSPLIT + s) * 2] - M);
      L += wgt * pml[(row * NSPLIT + s) * 2 + 1];
      o += wgt * po[((size_t)row * NSPLIT + s) * DH + d];
    }
    os[row >> 4][(row & 15) * DH + d] = rbf(o / L);
  }
  for (int i = tid; i < B * DFF; i += 512) hs[i / DFF][i % DFF] = bf1(h[i]);
  if (wave < B) {
    float t = 0.f;
    for (int k = lane; k < D; k += 64) t += rbf(bf1(x[wave * D + k]) * bf1(gw[k]));
#pragma unroll
    for (int o = 32; o >= 1; o >>= 1) t += __shfl_xor(t, o);
    if (lane == 0) gs[wave] = rbf(1.f / (1.f + __expf(-rbf(t))));
  }
  __syncthreads();
  float a[B][8] = {}, bb[B][8] = {};
  for (int k = tid; k < HQ * DH; k += 512) {
    u32x4 v = *(const u32x4*)(wo + (size_t)k * D + n0);
#pragma unroll
    for (int b = 0; b < B; ++b) {
      const float xv = os[b][k];
#pragma unroll
      for (int j = 0; j < 4; ++j) { a[b][2 * j] += xv * bflo(v[j]); a[b][2 * j + 1] += xv * bfhi(v[j]); }
    }
  }
  for (int k = tid; k < DFF; k += 512) {
    u32x4 v = *(const u32x4*)(w2 + (size_t)k * D + n0);
#pragma unroll
    for (int b = 0; b < B; ++b) {
      const float xv = hs[b][k];
#pragma unroll
      for (int j = 0; j < 4; ++j) { bb[b][2 * j] += xv * bflo(v[j]); bb[b][2 * j + 1] += xv * bfhi(v[j]); }
    }
  }
#pragma unroll
  for (int b = 0; b < B; ++b)
#pragma unroll
    for (int j = 0; j < 8; ++j) {
#pragma unroll
      for (int o = 32; o >= 1; o >>= 1) { a[b][j] += __shfl_xor(a[b][j], o); bb[b][j] += __shfl_xor(bb[b][j], o); }
      if (lane == 0) { red[wave][0][b][j] = a[b][j]; red[wave][1][b][j] = bb[b][j]; }
    }
  __syncthreads();
  if (tid < 32) {
    const int b = tid >> 3, j = tid & 7;
    float sa = 0.f, sb = 0.f;
#pragma unroll
    for (int w = 0; w < 8; ++w) { sa += red[w][0][b][j]; sb += red[w][1][b][j]; }
    const float g = gs[b];
    out[b * D + n0 + j] = tobf(rbf(g * rbf(sa)) + rbf(rbf(1.f - g) * rbf(sb)));
  }
}

extern "C" void kernel_launch(void* const* d_in, const int* in_sizes, int n_in, void* d_out, int out_size, void* d_ws,
                              size_t ws_size, hipStream_t stream) {
  const unsigned short* x = (const unsigned short*)d_in[0];
  const unsigned short* kc = (const unsigned short*)d_in[1];
  const unsigned short* vc = (const unsigned short*)d_in[2];
  const unsigned short* wq = (const unsigned short*)d_in[3];
  const unsigned short* wo = (const unsigned short*)d_in[4];
  const unsigned short* w1 = (const unsigned short*)d_in[6];
  const unsigned short* w2 = (const unsigned short*)d_in[7];
  const unsigned short* gw = (const unsigned short*)d_in[8];
  char* ws = (char*)d_ws;
  unsigned short* q = (unsigned short*)ws;
  unsigned short* h = (unsigned short*)(ws + 65536);
  float* pml = (float*)(ws + 131072);
  float* po = (float*)(ws + 262144);
  k_prep<<<64 + 28, 256, 0, stream>>>(x, wq, w1, q, h);
  k_attn<<<256, 512, 0, stream>>>(q, kc, vc, po, pml);
  k_final<<<56, 512, 0, stream>>>(x, gw, h, po, pml, wo, w2, (unsigned short*)d_out);
}
```

```cpp
#include <hip/hip_runtime.h>
#include <stdint.h>

typedef unsigned u32x4 __attribute__((ext_vector_type(4)));
typedef unsigned u32x2 __attribute__((ext_vector_type(2)));
typedef float f32x4 __attribute__((ext_vector_type(4)));
typedef float f32x2 __attribute__((ext_vector_type(2)));
typedef short s16x4 __attribute__((ext_vector_type(4)));
typedef __bf16 bf16x2 __attribute__((ext_vector_type(2)));
typedef __bf16 bf16x8 __attribute__((ext_vector_type(8)));
#define LAS __attribute__((address_space(3)))
typedef __amdgpu_buffer_rsrc_t rsrc_t;

constexpr int NB = 4, DM = 448, HKV = 8, SK = 8192, DH = 256, WIN = 2048, DFF = 1792, NQ = 4096;

#define MAPX 1
#define GKB 128
#define ROTM 1
#define QMAP 0
#define PRETOUCH 1

constexpr int WS_EPOCH = 0;
constexpr int WS_HQ = 4096;
constexpr int WS_HH = WS_HQ + NB * 1024 * 16;
constexpr int WS_H1S = WS_HH + NB * 448 * 16;
constexpr int WS_H1 = WS_H1S + NB * HKV * 16 * 16;
constexpr int WS_H2 = WS_H1 + NB * HKV * 64 * 32 * 16;
constexpr int WS_END = WS_H2 + NB * 56 * 64 * 64;

constexpr int L_WQ = 0;
constexpr int L_W1 = L_WQ + 14336;
constexpr int L_PART = L_W1 + 7168;
constexpr int L_QS = L_PART + 16384;
constexpr int L_HS = L_QS + 1024;
constexpr int L_OB = L_HS + 64;
constexpr int L_PB = L_OB + 128;
constexpr int L_ML = L_PB + 1024;
constexpr int L_FLAG = L_ML + 128;
constexpr int L_MISC = L_FLAG + 16;
constexpr int L_END = L_MISC + 16;

constexpr int AUX_NT = 2, AUX_SC1 = 16;
constexpr unsigned SPIN_CAP = 1u << 18;
constexpr float LOG2E = 1.4426950408889634f;

#if defined(__HIP_DEVICE_COMPILE__)
__device__ __forceinline__ rsrc_t mk_rsrc(const void* p, unsigned bytes) {
  return __builtin_amdgcn_make_buffer_rsrc((void*)p, 0, bytes, 0x00020000);
}
template <int AUX> __device__ __forceinline__ u32x4 ld16(rsrc_t r, int voff, int soff) {
  return __builtin_amdgcn_raw_buffer_load_b128(r, voff, soff, AUX);
}
__device__ __forceinline__ void st_unit(rsrc_t r, int voff, unsigned a, unsigned b, unsigned tag) {
  __builtin_amdgcn_raw_buffer_store_b128(u32x4{a, tag, b, tag}, r, voff, 0, AUX_SC1);
}
__device__ __forceinline__ float asf(unsigned u) { return __builtin_bit_cast(float, u); }
__device__ __forceinline__ unsigned asu(float f) { return __builtin_bit_cast(unsigned, f); }
__device__ __forceinline__ unsigned pkbf(float lo, float hi) {
  return __builtin_bit_cast(unsigned, __builtin_convertvector((f32x2{lo, hi}), bf16x2));
}
__device__ __forceinline__ float rbf(float f) { return asf(pkbf(f, f) << 16); }
__device__ __forceinline__ float dot2(unsigned a, unsigned b, float acc) {
  return __builtin_amdgcn_fdot2_f32_bf16(__builtin_bit_cast(bf16x2, a), __builtin_bit_cast(bf16x2, b), acc, false);
}
template <int CTRL> __device__ __forceinline__ float dpp(float x) {
  return asf(__builtin_amdgcn_mov_dpp(asu(x), CTRL, 0xf, 0xf, true));
}
constexpr int XOR1 = 0xB1, XOR2 = 0x4E, XOR7 = 0x141, ROR8 = 0x128, ROR4 = 0x124, BCAST = 0x150;
__device__ __forceinline__ float swap16_add(float a, float b) {
  auto s = __builtin_amdgcn_permlane16_swap(asu(a), asu(b), false, false);
  return asf(s[0]) + asf(s[1]);
}
__device__ __forceinline__ float swap32_add(float a, float b) {
  auto s = __builtin_amdgcn_permlane32_swap(asu(a), asu(b), false, false);
  return asf(s[0]) + asf(s[1]);
}
__device__ __forceinline__ float swap16_max(float a) {
  auto s = __builtin_amdgcn_permlane16_swap(asu(a), asu(a), false, false);
  return fmaxf(asf(s[0]), asf(s[1]));
}
__device__ __forceinline__ float swap32_max(float a) {
  auto s = __builtin_amdgcn_permlane32_swap(asu(a), asu(a), false, false);
  return fmaxf(asf(s[0]), asf(s[1]));
}
#define FENCE() __builtin_amdgcn_sched_barrier(0)
#define WAIT_VM0() asm volatile("s_waitcnt vmcnt(0)" ::: "memory")
#define WG_BARRIER() do { asm volatile("s_waitcnt lgkmcnt(0)\n\ts_barrier" ::: "memory"); } while (0)
template <typename T> __device__ __forceinline__ T lds_ld(LAS char* L, int off) { return *(LAS T*)(L + off); }
template <typename T> __device__ __forceinline__ void lds_st(LAS char* L, int off, T v) { *(LAS T*)(L + off) = v; }

#define TS(i) do {} while (0)

template <int ROWB> __device__ __forceinline__ f32x4 proj_tile(LAS char* L, int lbase, const u32x4 (&xf)[14], int lane) {
  const int g = lane >> 4, q = (lane & 15) >> 2, p = lane & 3;
  const int a0 = lbase + (8 * g + q) * ROWB + 8 * (ROWB == 32 ? p : (p & 1));
  f32x4 acc = {0.f, 0.f, 0.f, 0.f};
#pragma unroll
  for (int t = 0; t < 14; ++t) {
    s16x4 lo = __builtin_amdgcn_ds_read_tr16_b64_v4i16((LAS s16x4*)(L + a0 + t * 32 * ROWB));
    s16x4 hi = __builtin_amdgcn_ds_read_tr16_b64_v4i16((LAS s16x4*)(L + a0 + t * 32 * ROWB + 4 * ROWB));
    u32x2 l2 = __builtin_bit_cast(u32x2, lo), h2 = __builtin_bit_cast(u32x2, hi);
    u32x4 w = {l2[0], l2[1], h2[0], h2[1]};
    acc = __builtin_amdgcn_mfma_f32_16x16x32_bf16(__builtin_bit_cast(bf16x8, w), __builtin_bit_cast(bf16x8, xf[t]), acc, 0, 0, 0);
  }
  return acc;
}

__device__ __forceinline__ void qk_dots(const u32x4& k, const u32x4& q0, const u32x4& q1, float& d0, float& d1) {
  float a = 0.f, b = 0.f;
#pragma unroll
  for (int j = 0; j < 4; ++j) { a = dot2(k[j], q0[j], a); b = dot2(k[j], q1[j], b); }
  d0 = a; d1 = b;
}

__device__ __forceinline__ void softmax_wave(float (&x)[32], LAS char* L, int wave, int lane, u32x4 (&pp)[4]) {
#pragma unroll
  for (int v = 0; v < 16; ++v) x[v] = swap16_add(x[v], x[v + 16]);
#define TR_STEP(H, CTRL, BIT) _Pragma("unroll") for (int v = 0; v < (H); ++v) { \
    const float t1 = x[v] + dpp<CTRL>(x[v]), t2 = x[v + (H)] + dpp<CTRL>(x[v + (H)]); x[v] = (lane & (BIT)) ? t2 : t1; }
  TR_STEP(8, ROR8, 8) TR_STEP(4, XOR7, 4) TR_STEP(2, XOR2, 2) TR_STEP(1, XOR1, 1)
#undef TR_STEP
  const float t = rbf(x[0]) * (0.0625f * LOG2E);
  float m = fmaxf(t, dpp<XOR2>(t));
  m = fmaxf(m, dpp<ROR4>(m)); m = fmaxf(m, dpp<ROR8>(m)); m = swap32_max(swap16_max(m));
  const float p = __builtin_amdgcn_exp2f(t - (m == -INFINITY ? 0.f : m));
  const float po = dpp<XOR2>(p);
  float l = p + po;
  l += dpp<ROR4>(l); l += dpp<ROR8>(l); l = swap16_add(l, l); l = swap32_add(l, l);
  const int pb = L_PB + wave * 128;
  if (!(lane & 2)) lds_st<unsigned>(L, pb + (((lane >> 5) * 2 + (lane & 1)) * 8 + ((lane & 31) >> 2)) * 4, pkbf(p, po));
  if (lane < 2) lds_st<f32x2>(L, L_ML + wave * 16 + lane * 8, f32x2{m, l});
  asm volatile("s_waitcnt lgkmcnt(0)" ::: "memory");
#pragma unroll
  for (int j = 0; j < 4; ++j) pp[j] = lds_ld<u32x4>(L, pb + (lane >> 5) * 64 + j * 16);
}

__device__ __forceinline__ void pv_pair(const u32x4& v0, const u32x4& v1, unsigned p0, unsigned p1, float (&acc)[16]) {
#pragma unroll
  for (int k = 0; k < 4; ++k) {
    const unsigned lo = __builtin_amdgcn_perm(v1[k], v0[k], 0x05040100u), hi = __builtin_amdgcn_perm(v1[k], v0[k], 0x07060302u);
    acc[2 * k] = dot2(lo, p0, acc[2 * k]); acc[2 * k + 1] = dot2(hi, p0, acc[2 * k + 1]);
    acc[8 + 2 * k] = dot2(lo, p1, acc[8 + 2 * k]); acc[9 + 2 * k] = dot2(hi, p1, acc[9 + 2 * k]);
  }
}
__device__ __forceinline__ void store_part(float (&acc)[16], LAS char* L, int wave, int lane) {
  f32x4 a, b;
#pragma unroll
  for (int v = 0; v < 4; ++v) { a[v] = swap32_add(acc[v], acc[v + 8]); b[v] = swap32_add(acc[v + 4], acc[v + 12]); }
  lds_st<f32x4>(L, L_PART + wave * 2048 + lane * 32, a);
  lds_st<f32x4>(L, L_PART + wave * 2048 + lane * 32 + 16, b);
}
__device__ __forceinline__ void pair_rows(u32x4& r0, u32x4& r1) {
#pragma unroll
  for (int k = 0; k < 4; ++k) {
    const unsigned lo = __builtin_amdgcn_perm(r1[k], r0[k], 0x05040100u), hi = __builtin_amdgcn_perm(r1[k], r0[k], 0x07060302u);
    r0[k] = lo; r1[k] = hi;
  }
}
__device__ __forceinline__ void gemv_pair(const u32x4& lo, const u32x4& hi, unsigned a, float (&acc)[8]) {
#pragma unroll
  for (int k = 0; k < 4; ++k) { acc[2 * k] = dot2(lo[k], a, acc[2 * k]); acc[2 * k + 1] = dot2(hi[k], a, acc[2 * k + 1]); }
}
__device__ __forceinline__ bool unit_ok(const u32x4& u, unsigned tag) { return u[1] == tag && u[3] == tag; }
__device__ __forceinline__ int kb_of(int u, int s) { return (u / GKB) * (8 * GKB) + s * GKB + (u % GKB); }
#endif

__global__ void __launch_bounds__(512, 2)
fused_decode(const unsigned short* __restrict__ x, const unsigned short* __restrict__ kc, const unsigned short* __restrict__ vc,
             const unsigned short* __restrict__ wq, const unsigned short* __restrict__ wo, const unsigned short* __restrict__ w1,
             const unsigned short* __restrict__ w2, const unsigned short* __restrict__ gw, unsigned short* __restrict__ out,
             char* __restrict__ ws) {
#if defined(__HIP_DEVICE_COMPILE__)
  __shared__ __attribute__((aligned(16))) char smem[L_END];
  LAS char* L = (LAS char*)smem;
  const int lane = threadIdx.x & 63, wave = __builtin_amdgcn_readfirstlane(threadIdx.x >> 6);
  const int bid = blockIdx.x, xl = bid & 7, r = bid >> 3, b = r >> 3;
  const int kvh = MAPX ? xl : (r & 7), s = MAPX ? (r & 7) : xl;
  const int grp = b * HKV + kvh;
  const bool red = s < 7;
  const int jf = red ? kvh * 7 + s : 0;
  const size_t hb = ((size_t)grp * SK + (SK - WIN)) * DH;
  const rsrc_t rk = mk_rsrc(kc + hb, WIN * DH * 2), rv = mk_rsrc(vc + hb, WIN * DH * 2);
  const rsrc_t rw = mk_rsrc(ws, WS_END);
  const int rot = ROTM == 0 ? 0 : ((bid * 37) & 31) * 4;
  const int w4 = wave & 3;
#define KV_SOFF(G, i) (kb_of(((G) * 64 + 4 * (i) + w4 + rot) & 127, s) * 1024)
  TS(0);

  if (wave >= 4) {
    u32x4 K[16], V[16];
#pragma unroll
    for (int i = 0; i < 16; ++i) { K[i] = ld16<AUX_NT>(rk, lane * 16, KV_SOFF(0, i)); FENCE(); }
#pragma unroll
    for (int i = 0; i < 16; ++i) { V[i] = ld16<AUX_NT>(rv, lane * 16, KV_SOFF(0, i)); FENCE(); }
    TS(1);
    WG_BARRIER();
    TS(2);
    const u32x4 q0 = lds_ld<u32x4>(L, L_QS + (lane & 31) * 16), q1 = lds_ld<u32x4>(L, L_QS + 512 + (lane & 31) * 16);
    float sc[32];
#pragma unroll
    for (int i = 0; i < 16; ++i) qk_dots(K[i], q0, q1, sc[2 * i], sc[2 * i + 1]);
    u32x4 pp[4];
    softmax_wave(sc, L, wave, lane, pp);
    float acc[16];
#pragma unroll
    for (int v = 0; v < 16; ++v) acc[v] = 0.f;
#pragma unroll
    for (int u = 0; u < 8; ++u) pv_pair(V[2 * u], V[2 * u + 1], pp[u >> 2][u & 3], pp[2 + (u >> 2)][u & 3], acc);
    store_part(acc, L, wave, lane);
    const int rg = lane >> 4, c = lane & 15;
    const rsrc_t ro = mk_rsrc(wo + (size_t)(2 * kvh * DH + 32 * s) * DM, (DH + 32) * DM * 2);
    const rsrc_t r2 = mk_rsrc(w2 + (size_t)(32 * jf) * DM, red ? 32 * DM * 2 : 0);
    const int colb = c < 14 ? 224 * w4 + 16 * c : 0x40000000;
    const int vo = ((rg >> 1) * DH + (rg & 1) * 16) * (DM * 2) + colb, v2 = rg * 8 * (DM * 2) + colb;
    const u32x4 misc = lds_ld<u32x4>(L, L_MISC), hp = lds_ld<u32x4>(L, L_HS + rg * 16);
    TS(3);
    WG_BARRIER();
    TS(4);
    u32x4 W2[8], WO[16];
#pragma unroll
    for (int i = 0; i < 8; ++i) { W2[i] = ld16<AUX_NT>(r2, v2, i * DM * 2); FENCE(); }
#pragma unroll
    for (int i = 0; i < 16; ++i) { WO[i] = ld16<AUX_NT>(ro, vo, i * DM * 2); FENCE(); }
    TS(5);
    float af[8], aa[8];
#pragma unroll
    for (int v = 0; v < 8; ++v) { af[v] = 0.f; aa[v] = 0.f; }
#pragma unroll
    for (int u = 0; u < 4; ++u) { pair_rows(W2[2 * u], W2[2 * u + 1]); gemv_pair(W2[2 * u], W2[2 * u + 1], hp[u], af); }
#pragma unroll
    for (int v = 0; v < 8; ++v) af[v] *= asf(misc[2]);
#pragma unroll
    for (int u = 0; u < 8; ++u) { pair_rows(WO[2 * u], WO[2 * u + 1]); asm volatile("" : "+v"(WO[2 * u]), "+v"(WO[2 * u + 1])); }
    TS(6);
    WG_BARRIER();
    TS(7);
    const u32x4 o0 = lds_ld<u32x4>(L, L_OB + rg * 32), o1 = lds_ld<u32x4>(L, L_OB + rg * 32 + 16);
#pragma unroll
    for (int u = 0; u < 8; ++u) gemv_pair(WO[2 * u], WO[2 * u + 1], u < 4 ? o0[u & 3] : o1[u & 3], aa);
#pragma unroll
    for (int v = 0; v < 8; ++v) aa[v] = fmaf(aa[v], asf(misc[1]), af[v]);
    const float y0 = swap16_add(aa[0], aa[2]), y1 = swap16_add(aa[1], aa[3]), y2 = swap16_add(aa[4], aa[6]), y3 = swap16_add(aa[5], aa[7]);
    const float z0 = swap32_add(y0, y2), z1 = swap32_add(y1, y3);
    if (c < 14) st_unit(rw, WS_H2 + (((b * 56 + 14 * w4 + c) * 64 + kvh * 8 + s) * 4 + rg) * 16, asu(z0), asu(z1), misc[0]);
    TS(8);
    return;
  }

  unsigned tag = 0;
  if (wave < 2) {
    const int lgh = xl + 8 * (r >> 3);
    if (wave == 0 || lgh < 28) {
      const int lg = QMAP ? 8 * xl + (r >> 2) : 8 * (r >> 2) + ((xl + (r >> 2)) & 7);
      const int cq = 4 * lg + (r & 3), ch = 8 * lgh + (r & 7);
      const rsrc_t rx = mk_rsrc(x, NB * DM * 2);
      if (wave == 0) {
        const rsrc_t rq = mk_rsrc(wq + 16 * cq, ((DM - 1) * NQ + 16) * 2);
#pragma unroll
        for (int i = 0; i < 14; ++i)
          __builtin_amdgcn_raw_ptr_buffer_load_lds(rq, (LAS void*)(L + L_WQ + i * 1024), 16, (lane >> 1) * (NQ * 2) + (lane & 1) * 16, i * 32 * NQ * 2, 0, AUX_NT);
      } else {
        const rsrc_t r1 = mk_rsrc(w1 + 8 * ch, ((DM - 1) * DFF + 8) * 2);
#pragma unroll
        for (int i = 0; i < 7; ++i)
          __builtin_amdgcn_raw_ptr_buffer_load_lds(r1, (LAS void*)(L + L_W1 + i * 1024), 16, lane * (DFF * 2), i * 64 * DFF * 2, 0, AUX_NT);
      }
      u32x4 xf[14];
#pragma unroll
      for (int t = 0; t < 14; ++t) xf[t] = ld16<0>(rx, (lane & 15) * (DM * 2) + (lane >> 4) * 16, t * 64);
      tag = __builtin_amdgcn_raw_buffer_load_b32(rw, WS_EPOCH, 0, AUX_SC1) + 1u;
      WAIT_VM0();
      FENCE();
      TS(1);
      const int n = lane & 15, g = lane >> 4;
      if (wave == 0) {
        const f32x4 a = proj_tile<32>(L, L_WQ, xf, lane);
        if (n < NB) st_unit(rw, WS_HQ + (n * 1024 + cq * 4 + g) * 16, pkbf(a[0], a[1]), pkbf(a[2], a[3]), tag);
      } else {
        f32x4 a = proj_tile<16>(L, L_W1, xf, lane);
#pragma unroll
        for (int v = 0; v < 4; ++v) a[v] = a[v] < 0.f ? 0.f : a[v];
        if (n < NB && g < 2) st_unit(rw, WS_HH + (n * 448 + ch * 2 + g) * 16, pkbf(a[0], a[1]), pkbf(a[2], a[3]), tag);
      }
      TS(2);
    }
    if (wave == 0) {
      u32x4 a0, a1, a2;
      const int oq = WS_HQ + (b * 1024 + kvh * 128 + lane) * 16, oh = WS_HH + (b * 448 + jf * 8 + (lane & 7)) * 16;
      for (unsigned it = 0; it < SPIN_CAP; ++it) {
        asm volatile("" ::: "memory");
        a0 = ld16<AUX_SC1>(rw, oq, 0); a1 = ld16<AUX_SC1>(rw, oq, 1024); a2 = ld16<AUX_SC1>(rw, oh, 0);
        if (__all(unit_ok(a0, tag) && unit_ok(a1, tag) && unit_ok(a2, tag))) break;
      }
      lds_st<u32x2>(L, L_QS + lane * 8, u32x2{a0[0], a0[2]});
      lds_st<u32x2>(L, L_QS + 512 + lane * 8, u32x2{a1[0], a1[2]});
      if (lane < 8) lds_st<u32x2>(L, L_HS + lane * 8, red ? u32x2{a2[0], a2[2]} : u32x2{0u, 0u});
      if (lane == 0) lds_st<unsigned>(L, L_MISC, tag);
      TS(3);
    }
  } else if (wave == 2) {
    const rsrc_t rx = mk_rsrc(x + b * DM, DM * 2), rg = mk_rsrc(gw, DM * 2);
    const u32x4 xv = ld16<0>(rx, lane * 16, 0), gv = ld16<0>(rg, lane * 16, 0);
    float t = 0.f;
#pragma unroll
    for (int k = 0; k < 4; ++k) {
      t += rbf(asf(xv[k] << 16) * asf(gv[k] << 16));
      t += rbf(asf(xv[k] & 0xffff0000u) * asf(gv[k] & 0xffff0000u));
    }
    t += dpp<XOR1>(t); t += dpp<XOR2>(t); t += dpp<ROR4>(t); t += dpp<ROR8>(t);
    t = swap16_add(t, t); t = swap32_add(t, t);
    const float g = rbf(1.f / (1.f + __expf(-rbf(t))));
    if (lane == 0) { lds_st<float>(L, L_MISC + 4, g); lds_st<float>(L, L_MISC + 8, rbf(1.f - g)); }
    if (lane < 4) lds_st<unsigned>(L, L_FLAG + lane * 4, 0u);
  } else if (PRETOUCH) {
    const rsrc_t ro = mk_rsrc(wo + (size_t)(2 * kvh * DH + 32 * s) * DM, (DH + 32) * DM * 2);
    const rsrc_t r2 = mk_rsrc(w2 + (size_t)(32 * jf) * DM, red ? 32 * DM * 2 : 0);
    unsigned t0 = __builtin_amdgcn_raw_buffer_load_b32(rv, lane == 0 ? 0 : 0x40000000, KV_SOFF(0, 0), AUX_NT);
    unsigned t1 = __builtin_amdgcn_raw_buffer_load_b32(ro, lane < 2 ? lane * DH * DM * 2 : 0x40000000, 0, AUX_NT);
    unsigned t2 = __builtin_amdgcn_raw_buffer_load_b32(r2, lane == 0 ? 0 : 0x40000000, 0, AUX_NT);
    unsigned t3 = __builtin_amdgcn_raw_buffer_load_b32(rw, lane == 0 ? WS_H1 + (grp * 8 + s) * 4096 : lane == 1 ? WS_H2 + (b * 56 + jf) * 4096 : 0x40000000, 0, AUX_SC1);
    asm volatile("" :: "v"(t0), "v"(t1), "v"(t2), "v"(t3));
  }
  TS(4);
  WG_BARRIER();
  TS(5);
  {
    u32x4 K[16], V[16];
#pragma unroll
    for (int i = 0; i < 16; ++i) { K[i] = ld16<AUX_NT>(rk, lane * 16, KV_SOFF(1, i)); FENCE(); }
    const u32x4 q0 = lds_ld<u32x4>(L, L_QS + (lane & 31) * 16), q1 = lds_ld<u32x4>(L, L_QS + 512 + (lane & 31) * 16);
    tag = lds_ld<unsigned>(L, L_MISC);
    float sc[32];
#pragma unroll
    for (int i = 0; i < 8; ++i) {
      V[i] = ld16<AUX_NT>(rv, lane * 16, KV_SOFF(1, i)); FENCE();
      qk_dots(K[2 * i], q0, q1, sc[4 * i], sc[4 * i + 1]);
      qk_dots(K[2 * i + 1], q0, q1, sc[4 * i + 2], sc[4 * i + 3]);
      FENCE();
    }
#pragma unroll
    for (int i = 8; i < 12; ++i) { V[i] = ld16<AUX_NT>(rv, lane * 16, KV_SOFF(1, i)); FENCE(); }
    TS(6);
    u32x4 pp[4];
    softmax_wave(sc, L, wave, lane, pp);
    float acc[16];
#pragma unroll
    for (int v = 0; v < 16; ++v) acc[v] = 0.f;
    FENCE();
#pragma unroll
    for (int u = 0; u < 2; ++u) {
      V[12 + 2 * u] = ld16<AUX_NT>(rv, lane * 16, KV_SOFF(1, 12 + 2 * u)); FENCE();
      V[13 + 2 * u] = ld16<AUX_NT>(rv, lane * 16, KV_SOFF(1, 13 + 2 * u)); FENCE();
      if (u == 0) { pv_pair(V[0], V[1], pp[0][0], pp[2][0], acc); FENCE(); }
    }
    TS(7);
    WG_BARRIER();
    TS(8);
    const int hd = wave >> 1;
    float wt[8], M = -INFINITY, Ls = 0.f;
    f32x2 ml[8];
#pragma unroll
    for (int w = 0; w < 8; ++w) { ml[w] = lds_ld<f32x2>(L, L_ML + w * 16 + hd * 8); M = fmaxf(M, ml[w][0]); }
#pragma unroll
    for (int w = 0; w < 8; ++w) { wt[w] = __builtin_amdgcn_exp2f(ml[w][0] - (M == -INFINITY ? 0.f : M)); Ls = fmaf(wt[w], ml[w][1], Ls); }
    const int pf = L_PART + wave * 512 + lane * 8;
    f32x2 sum = {0.f, 0.f};
#pragma unroll
    for (int w = 4; w < 8; ++w) { const f32x2 t = lds_ld<f32x2>(L, pf + w * 2048); sum[0] = fmaf(wt[w], t[0], sum[0]); sum[1] = fmaf(wt[w], t[1], sum[1]); }
    FENCE();
#pragma unroll
    for (int u = 1; u < 8; ++u) { pv_pair(V[2 * u], V[2 * u + 1], pp[u >> 2][u & 3], pp[2 + (u >> 2)][u & 3], acc); FENCE(); }
    TS(9);
    store_part(acc, L, wave, lane);
    asm volatile("s_waitcnt lgkmcnt(0)" ::: "memory");
    if (lane == 0) lds_st<unsigned>(L, L_FLAG + wave * 4, 1u);
    for (unsigned it = 0; it < SPIN_CAP; ++it) {
      asm volatile("" ::: "memory");
      const u32x4 f = lds_ld<u32x4>(L, L_FLAG);
      if (__builtin_amdgcn_readfirstlane(f[0] & f[1] & f[2] & f[3])) break;
    }
    asm volatile("" ::: "memory");
#pragma unroll
    for (int w = 0; w < 4; ++w) { const f32x2 t = lds_ld<f32x2>(L, pf + w * 2048); sum[0] = fmaf(wt[w], t[0], sum[0]); sum[1] = fmaf(wt[w], t[1], sum[1]); }
    const int dst = (wave & 1) * 4 + (lane >> 4);
    st_unit(rw, WS_H1 + (((grp * 8 + dst) * 8 + s) * 32 + hd * 16 + (lane & 15)) * 16, asu(sum[0]), asu(sum[1]), tag);
    if (!(wave & 1) && lane == 0) st_unit(rw, WS_H1S + (grp * 16 + s * 2 + hd) * 16, asu(M), asu(Ls), tag);
    TS(10);
  }
  if (wave == 0) {
    u32x4 a[4], st;
    const int oa = WS_H1 + (grp * 8 + s) * 4096 + lane * 16, os = WS_H1S + (grp * 16 + (((lane & 15) + (lane >> 4)) & 15)) * 16;
    for (unsigned it = 0; it < SPIN_CAP; ++it) {
      asm volatile("" ::: "memory");
      st = ld16<AUX_SC1>(rw, os, 0);
#pragma unroll
      for (int i = 0; i < 4; ++i) a[i] = ld16<AUX_SC1>(rw, oa, i * 1024);
      if (__all(unit_ok(st, tag) && unit_ok(a[0], tag) && unit_ok(a[1], tag) && unit_ok(a[2], tag) && unit_ok(a[3], tag))) break;
    }
    TS(11);
    const float Ms = asf(st[0]);
    float Mg = fmaxf(Ms, dpp<XOR2>(Ms)); Mg = fmaxf(Mg, dpp<ROR4>(Mg)); Mg = fmaxf(Mg, dpp<ROR8>(Mg));
    const float e = __builtin_amdgcn_exp2f(Ms - Mg);
    float Lg = e * asf(st[2]);
    Lg += dpp<XOR2>(Lg); Lg += dpp<ROR4>(Lg); Lg += dpp<ROR8>(Lg);
    const float wgt = e * __builtin_amdgcn_rcpf(Lg);
    const float w0 = dpp<BCAST + 0>(wgt), w1_ = dpp<BCAST + 4>(wgt), w2_ = dpp<BCAST + 8>(wgt), w3 = dpp<BCAST + 12>(wgt);
    float o0 = w0 * asf(a[0][0]) + w1_ * asf(a[1][0]) + w2_ * asf(a[2][0]) + w3 * asf(a[3][0]);
    float o1 = w0 * asf(a[0][2]) + w1_ * asf(a[1][2]) + w2_ * asf(a[2][2]) + w3 * asf(a[3][2]);
    o0 = swap32_add(o0, o0); o1 = swap32_add(o1, o1);
    if (lane < 32) lds_st<unsigned>(L, L_OB + lane * 4, pkbf(o0, o1));
  }
  WG_BARRIER();
  TS(12);
  if (wave == 0 && red) {
    u32x4 a[4];
    const int oa = WS_H2 + (b * 56 + jf) * 4096 + lane * 16;
    for (unsigned it = 0; it < SPIN_CAP; ++it) {
      asm volatile("" ::: "memory");
#pragma unroll
      for (int i = 0; i < 4; ++i) a[i] = ld16<AUX_SC1>(rw, oa, i * 1024);
      if (__all(unit_ok(a[0], tag) && unit_ok(a[1], tag) && unit_ok(a[2], tag) && unit_ok(a[3], tag))) break;
    }
    TS(13);
    float o0 = (asf(a[0][0]) + asf(a[1][0])) + (asf(a[2][0]) + asf(a[3][0]));
    float o1 = (asf(a[0][2]) + asf(a[1][2])) + (asf(a[2][2]) + asf(a[3][2]));
    o0 += dpp<ROR4>(o0); o1 += dpp<ROR4>(o1); o0 += dpp<ROR8>(o0); o1 += dpp<ROR8>(o1);
    o0 = swap16_add(o0, o0); o1 = swap16_add(o1, o1); o0 = swap32_add(o0, o0); o1 = swap32_add(o1, o1);
    if (lane < 4) *(unsigned*)(out + b * DM + 8 * jf + 2 * lane) = pkbf(o0, o1);
    if (bid == 0 && lane == 0) __builtin_amdgcn_raw_buffer_store_b32(tag, rw, WS_EPOCH, 0, AUX_SC1);
    TS(14);
  }
#endif
}

extern "C" void kernel_launch(void* const* d_in, const int* in_sizes, int n_in, void* d_out, int out_size, void* d_ws,
                              size_t ws_size, hipStream_t stream) {
  typedef const unsigned short* P;
  fused_decode<<<256, 512, 0, stream>>>((P)d_in[0], (P)d_in[1], (P)d_in[2], (P)d_in[3], (P)d_in[4], (P)d_in[6], (P)d_in[7],
                                        (P)d_in[8], (unsigned short*)d_out, (char*)d_ws);
}
```

```cpp
#define PROBE_Q 0
#define PROBE_W 0
#define PROBE_K 1
#include <hip/hip_runtime.h>
#include <stdint.h>

typedef unsigned u32x4 __attribute__((ext_vector_type(4)));
typedef unsigned u32x2 __attribute__((ext_vector_type(2)));
typedef float f32x4 __attribute__((ext_vector_type(4)));
typedef float f32x2 __attribute__((ext_vector_type(2)));
typedef short s16x4 __attribute__((ext_vector_type(4)));
typedef __bf16 bf16x2 __attribute__((ext_vector_type(2)));
typedef __bf16 bf16x8 __attribute__((ext_vector_type(8)));
#define LAS __attribute__((address_space(3)))
typedef __amdgpu_buffer_rsrc_t rsrc_t;

constexpr int NB = 4, DM = 448, HKV = 8, SK = 8192, DH = 256, WIN = 2048, DFF = 1792, NQ = 4096;

#define MAPX 1
#define GKB 128
#define ROTM 1
#define QMAP 0
#define PRETOUCH 1

constexpr int WS_EPOCH = 0;
constexpr int WS_HQ = 4096;
constexpr int WS_HH = WS_HQ + NB * 1024 * 16;
constexpr int WS_H1S = WS_HH + NB * 448 * 16;
constexpr int WS_H1 = WS_H1S + NB * HKV * 16 * 16;
constexpr int WS_H2 = WS_H1 + NB * HKV * 64 * 32 * 16;
constexpr int WS_END = WS_H2 + NB * 56 * 64 * 64;

constexpr int L_WQ = 0;
constexpr int L_W1 = L_WQ + 14336;
constexpr int L_PART = L_W1 + 7168;
constexpr int L_QS = L_PART + 16384;
constexpr int L_HS = L_QS + 1024;
constexpr int L_OB = L_HS + 64;
constexpr int L_PB = L_OB + 128;
constexpr int L_ML = L_PB + 1024;
constexpr int L_FLAG = L_ML + 128;
constexpr int L_MISC = L_FLAG + 16;
constexpr int L_END = L_MISC + 16;

constexpr int AUX_NT = 2, AUX_SC1 = 16;
constexpr unsigned SPIN_CAP = 1u << 18;
constexpr float LOG2E = 1.4426950408889634f;

#if defined(__HIP_DEVICE_COMPILE__)
__device__ __forceinline__ rsrc_t mk_rsrc(const void* p, unsigned bytes) {
  return __builtin_amdgcn_make_buffer_rsrc((void*)p, 0, bytes, 0x00020000);
}
template <int AUX> __device__ __forceinline__ u32x4 ld16(rsrc_t r, int voff, int soff) {
  return __builtin_amdgcn_raw_buffer_load_b128(r, voff, soff, AUX);
}
__device__ __forceinline__ void st_unit(rsrc_t r, int voff, unsigned a, unsigned b, unsigned tag) {
  __builtin_amdgcn_raw_buffer_store_b128(u32x4{a, tag, b, tag}, r, voff, 0, AUX_SC1);
}
__device__ __forceinline__ float asf(unsigned u) { return __builtin_bit_cast(float, u); }
__device__ __forceinline__ unsigned asu(float f) { return __builtin_bit_cast(unsigned, f); }
__device__ __forceinline__ unsigned pkbf(float lo, float hi) {
  return __builtin_bit_cast(unsigned, __builtin_convertvector((f32x2{lo, hi}), bf16x2));
}
__device__ __forceinline__ float rbf(float f) { return asf(pkbf(f, f) << 16); }
__device__ __forceinline__ float dot2(unsigned a, unsigned b, float acc) {
  return __builtin_amdgcn_fdot2_f32_bf16(__builtin_bit_cast(bf16x2, a), __builtin_bit_cast(bf16x2, b), acc, false);
}
template <int CTRL> __device__ __forceinline__ float dpp(float x) {
  return asf(__builtin_amdgcn_mov_dpp(asu(x), CTRL, 0xf, 0xf, true));
}
constexpr int XOR1 = 0xB1, XOR2 = 0x4E, XOR7 = 0x141, ROR8 = 0x128, ROR4 = 0x124, BCAST = 0x150;
__device__ __forceinline__ float swap16_add(float a, float b) {
  auto s = __builtin_amdgcn_permlane16_swap(asu(a), asu(b), false, false);
  return asf(s[0]) + asf(s[1]);
}
__device__ __forceinline__ float swap32_add(float a, float b) {
  auto s = __builtin_amdgcn_permlane32_swap(asu(a), asu(b), false, false);
  return asf(s[0]) + asf(s[1]);
}
__device__ __forceinline__ float swap16_max(float a) {
  auto s = __builtin_amdgcn_permlane16_swap(asu(a), asu(a), false, false);
  return fmaxf(asf(s[0]), asf(s[1]));
}
__device__ __forceinline__ float swap32_max(float a) {
  auto s = __builtin_amdgcn_permlane32_swap(asu(a), asu(a), false, false);
  return fmaxf(asf(s[0]), asf(s[1]));
}
#define FENCE() __builtin_amdgcn_sched_barrier(0)
#define WAIT_VM0() asm volatile("s_waitcnt vmcnt(0)" ::: "memory")
#define WG_BARRIER() do { asm volatile("s_waitcnt lgkmcnt(0)\n\ts_barrier" ::: "memory"); } while (0)
template <typename T> __device__ __forceinline__ T lds_ld(LAS char* L, int off) { return *(LAS T*)(L + off); }
template <typename T> __device__ __forceinline__ void lds_st(LAS char* L, int off, T v) { *(LAS T*)(L + off) = v; }

#define TS_DECL unsigned long long tsv[16] = {}
#define TS_ON(i) ((i) == 0 || (i) == PROBE_K)
#define TS(i) do { if (TS_ON(i)) { FENCE(); asm volatile("s_memrealtime %0\n\ts_waitcnt lgkmcnt(0)" : "=s"(tsv[i]) :: "memory"); FENCE(); } } while (0)
#define TS_FLUSH() do { if (lane == 0) { _Pragma("unroll") for (int i_ = 0; i_ < 16; ++i_) if (TS_ON(i_)) \
    __hip_atomic_store((unsigned long long*)(ws + (16 << 20)) + (blockIdx.x * 8 + wave) * 16 + i_, tsv[i_], __ATOMIC_RELAXED, __HIP_MEMORY_SCOPE_AGENT); } } while (0)
__device__ __forceinline__ unsigned long long rt_now() { unsigned long long t; asm volatile("s_memrealtime %0\n\ts_waitcnt lgkmcnt(0)" : "=s"(t) :: "memory"); return t; }
__device__ __forceinline__ void probe_tail(char* ws, unsigned long long own0, int lane) {
  while (rt_now() < own0 + 6000) {}
  unsigned long long lo = ~0ull, mx = 0, mn = ~0ull, sum = 0; unsigned cnt = 0;
  for (int w = lane; w < 256; w += 64) {
    for (int v = 0; v < 8; ++v) { unsigned long long t = __hip_atomic_load((unsigned long long*)(ws + (16 << 20)) + (w * 8 + v) * 16, __ATOMIC_RELAXED, __HIP_MEMORY_SCOPE_AGENT); lo = t < lo ? t : lo; }
    unsigned long long t = __hip_atomic_load((unsigned long long*)(ws + (16 << 20)) + (w * 8 + PROBE_W) * 16 + PROBE_K, __ATOMIC_RELAXED, __HIP_MEMORY_SCOPE_AGENT);
    if (t) { mx = t > mx ? t : mx; mn = t < mn ? t : mn; sum += t - (own0 - 10000); ++cnt; }
  }
  for (int o = 32; o >= 1; o >>= 1) {
    unsigned long long a = __shfl_xor(lo, o), b = __shfl_xor(mx, o), c = __shfl_xor(mn, o), d = __shfl_xor(sum, o); unsigned e = __shfl_xor(cnt, o);
    lo = a < lo ? a : lo; mx = b > mx ? b : mx; mn = c < mn ? c : mn; sum += d; cnt += e;
  }
  long long q = PROBE_Q == 0 ? 0 : PROBE_Q == 1 ? (long long)(mx - lo) : PROBE_Q == 2 ? (long long)(mn - lo) : (long long)(sum / cnt + (own0 - 10000) - lo);
  if (q < 0) q = 0;
  if (q > 5000) q = 5000;
  const unsigned long long end = lo + 10000 + 10 * q;
  for (int it = 0; it < 2000000 && rt_now() < end; ++it) {}
}

template <int ROWB> __device__ __forceinline__ f32x4 proj_tile(LAS char* L, int lbase, const u32x4 (&xf)[14], int lane) {
  const int g = lane >> 4, q = (lane & 15) >> 2, p = lane & 3;
  const int a0 = lbase + (8 * g + q) * ROWB + 8 * (ROWB == 32 ? p : (p & 1));
  f32x4 acc = {0.f, 0.f, 0.f, 0.f};
#pragma unroll
  for (int t = 0; t < 14; ++t) {
    s16x4 lo = __builtin_amdgcn_ds_read_tr16_b64_v4i16((LAS s16x4*)(L + a0 + t * 32 * ROWB));
    s16x4 hi = __builtin_amdgcn_ds_read_tr16_b64_v4i16((LAS s16x4*)(L + a0 + t * 32 * ROWB + 4 * ROWB));
    u32x2 l2 = __builtin_bit_cast(u32x2, lo), h2 = __builtin_bit_cast(u32x2, hi);
    u32x4 w = {l2[0], l2[1], h2[0], h2[1]};
    acc = __builtin_amdgcn_mfma_f32_16x16x32_bf16(__builtin_bit_cast(bf16x8, w), __builtin_bit_cast(bf16x8, xf[t]), acc, 0, 0, 0);
  }
  return acc;
}

__device__ __forceinline__ void qk_dots(const u32x4& k, const u32x4& q0, const u32x4& q1, float& d0, float& d1) {
  float a = 0.f, b = 0.f;
#pragma unroll
  for (int j = 0; j < 4; ++j) { a = dot2(k[j], q0[j], a); b = dot2(k[j], q1[j], b); }
  d0 = a; d1 = b;
}

__device__ __forceinline__ void softmax_wave(float (&x)[32], LAS char* L, int wave, int lane, u32x4 (&pp)[4]) {
#pragma unroll
  for (int v = 0; v < 16; ++v) x[v] = swap16_add(x[v], x[v + 16]);
#define TR_STEP(H, CTRL, BIT) _Pragma("unroll") for (int v = 0; v < (H); ++v) { \
    const float t1 = x[v] + dpp<CTRL>(x[v]), t2 = x[v + (H)] + dpp<CTRL>(x[v + (H)]); x[v] = (lane & (BIT)) ? t2 : t1; }
  TR_STEP(8, ROR8, 8) TR_STEP(4, XOR7, 4) TR_STEP(2, XOR2, 2) TR_STEP(1, XOR1, 1)
#undef TR_STEP
  const float t = rbf(x[0]) * (0.0625f * LOG2E);
  float m = fmaxf(t, dpp<XOR2>(t));
  m = fmaxf(m, dpp<ROR4>(m)); m = fmaxf(m, dpp<ROR8>(m)); m = swap32_max(swap16_max(m));
  const float p = __builtin_amdgcn_exp2f(t - (m == -INFINITY ? 0.f : m));
  const float po = dpp<XOR2>(p);
  float l = p + po;
  l += dpp<ROR4>(l); l += dpp<ROR8>(l); l = swap16_add(l, l); l = swap32_add(l, l);
  const int pb = L_PB + wave * 128;
  if (!(lane & 2)) lds_st<unsigned>(L, pb + (((lane >> 5) * 2 + (lane & 1)) * 8 + ((lane & 31) >> 2)) * 4, pkbf(p, po));
  if (lane < 2) lds_st<f32x2>(L, L_ML + wave * 16 + lane * 8, f32x2{m, l});
  asm volatile("s_waitcnt lgkmcnt(0)" ::: "memory");
#pragma unroll
  for (int j = 0; j < 4; ++j) pp[j] = lds_ld<u32x4>(L, pb + (lane >> 5) * 64 + j * 16);
}

__device__ __forceinline__ void pv_pair(const u32x4& v0, const u32x4& v1, unsigned p0, unsigned p1, float (&acc)[16]) {
#pragma unroll
  for (int k = 0; k < 4; ++k) {
    const unsigned lo = __builtin_amdgcn_perm(v1[k], v0[k], 0x05040100u), hi = __builtin_amdgcn_perm(v1[k], v0[k], 0x07060302u);
    acc[2 * k] = dot2(lo, p0, acc[2 * k]); acc[2 * k + 1] = dot2(hi, p0, acc[2 * k + 1]);
    acc[8 + 2 * k] = dot2(lo, p1, acc[8 + 2 * k]); acc[9 + 2 * k] = dot2(hi, p1, acc[9 + 2 * k]);
  }
}
__device__ __forceinline__ void store_part(float (&acc)[16], LAS char* L, int wave, int lane) {
  f32x4 a, b;
#pragma unroll
  for (int v = 0; v < 4; ++v) { a[v] = swap32_add(acc[v], acc[v + 8]); b[v] = swap32_add(acc[v + 4], acc[v + 12]); }
  lds_st<f32x4>(L, L_PART + wave * 2048 + lane * 32, a);
  lds_st<f32x4>(L, L_PART + wave * 2048 + lane * 32 + 16, b);
}
__device__ __forceinline__ void pair_rows(u32x4& r0, u32x4& r1) {
#pragma unroll
  for (int k = 0; k < 4; ++k) {
    const unsigned lo = __builtin_amdgcn_perm(r1[k], r0[k], 0x05040100u), hi = __builtin_amdgcn_perm(r1[k], r0[k], 0x07060302u);
    r0[k] = lo; r1[k] = hi;
  }
}
__device__ __forceinline__ void gemv_pair(const u32x4& lo, const u32x4& hi, unsigned a, float (&acc)[8]) {
#pragma unroll
  for (int k = 0; k < 4; ++k) { acc[2 * k] = dot2(lo[k], a, acc[2 * k]); acc[2 * k + 1] = dot2(hi[k], a, acc[2 * k + 1]); }
}
__device__ __forceinline__ bool unit_ok(const u32x4& u, unsigned tag) { return u[1] == tag && u[3] == tag; }
__device__ __forceinline__ int kb_of(int u, int s) { return (u / GKB) * (8 * GKB) + s * GKB + (u % GKB); }
#endif

__global__ void __launch_bounds__(512, 2)
fused_decode(const unsigned short* __restrict__ x, const unsigned short* __restrict__ kc, const unsigned short* __restrict__ vc,
             const unsigned short* __restrict__ wq, const unsigned short* __restrict__ wo, const unsigned short* __restrict__ w1,
             const unsigned short* __restrict__ w2, const unsigned short* __restrict__ gw, unsigned short* __restrict__ out,
             char* __restrict__ ws) {
#if defined(__HIP_DEVICE_COMPILE__)
  __shared__ __attribute__((aligned(16))) char smem[L_END];
  LAS char* L = (LAS char*)smem;
  const int lane = threadIdx.x & 63, wave = __builtin_amdgcn_readfirstlane(threadIdx.x >> 6);
  const int bid = blockIdx.x, xl = bid & 7, r = bid >> 3, b = r >> 3;
  const int kvh = MAPX ? xl : (r & 7), s = MAPX ? (r & 7) : xl;
  const int grp = b * HKV + kvh;
  const bool red = s < 7;
  const int jf = red ? kvh * 7 + s : 0;
  const size_t hb = ((size_t)grp * SK + (SK - WIN)) * DH;
  const rsrc_t rk = mk_rsrc(kc + hb, WIN * DH * 2), rv = mk_rsrc(vc + hb, WIN * DH * 2);
  const rsrc_t rw = mk_rsrc(ws, WS_END);
  const int rot = ROTM == 0 ? 0 : ((bid * 37) & 31) * 4;
  const int w4 = wave & 3;
#define KV_SOFF(G, i) (kb_of(((G) * 64 + 4 * (i) + w4 + rot) & 127, s) * 1024)
  TS_DECL;
  TS(0);

  if (wave >= 4) {
    u32x4 K[16], V[16];
#pragma unroll
    for (int i = 0; i < 16; ++i) { K[i] = ld16<AUX_NT>(rk, lane * 16, KV_SOFF(0, i)); FENCE(); }
#pragma unroll
    for (int i = 0; i < 16; ++i) { V[i] = ld16<AUX_NT>(rv, lane * 16, KV_SOFF(0, i)); FENCE(); }
    TS(1);
    WG_BARRIER();
    TS(2);
    const u32x4 q0 = lds_ld<u32x4>(L, L_QS + (lane & 31) * 16), q1 = lds_ld<u32x4>(L, L_QS + 512 + (lane & 31) * 16);
    float sc[32];
#pragma unroll
    for (int i = 0; i < 16; ++i) qk_dots(K[i], q0, q1, sc[2 * i], sc[2 * i + 1]);
    u32x4 pp[4];
    softmax_wave(sc, L, wave, lane, pp);
    float acc[16];
#pragma unroll
    for (int v = 0; v < 16; ++v) acc[v] = 0.f;
#pragma unroll
    for (int u = 0; u < 8; ++u) pv_pair(V[2 * u], V[2 * u + 1], pp[u >> 2][u & 3], pp[2 + (u >> 2)][u & 3], acc);
    store_part(acc, L, wave, lane);
    const int rg = lane >> 4, c = lane & 15;
    const rsrc_t ro = mk_rsrc(wo + (size_t)(2 * kvh * DH + 32 * s) * DM, (DH + 32) * DM * 2);
    const rsrc_t r2 = mk_rsrc(w2 + (size_t)(32 * jf) * DM, red ? 32 * DM * 2 : 0);
    const int colb = c < 14 ? 224 * w4 + 16 * c : 0x40000000;
    const int vo = ((rg >> 1) * DH + (rg & 1) * 16) * (DM * 2) + colb, v2 = rg * 8 * (DM * 2) + colb;
    const u32x4 misc = lds_ld<u32x4>(L, L_MISC), hp = lds_ld<u32x4>(L, L_HS + rg * 16);
    TS(3);
    WG_BARRIER();
    TS(4);
    u32x4 W2[8], WO[16];
#pragma unroll
    for (int i = 0; i < 8; ++i) { W2[i] = ld16<AUX_NT>(r2, v2, i * DM * 2); FENCE(); }
#pragma unroll
    for (int i = 0; i < 16; ++i) { WO[i] = ld16<AUX_NT>(ro, vo, i * DM * 2); FENCE(); }
    TS(5);
    float af[8], aa[8];
#pragma unroll
    for (int v = 0; v < 8; ++v) { af[v] = 0.f; aa[v] = 0.f; }
#pragma unroll
    for (int u = 0; u < 4; ++u) { pair_rows(W2[2 * u], W2[2 * u + 1]); gemv_pair(W2[2 * u], W2[2 * u + 1], hp[u], af); }
#pragma unroll
    for (int v = 0; v < 8; ++v) af[v] *= asf(misc[2]);
#pragma unroll
    for (int u = 0; u < 8; ++u) { pair_rows(WO[2 * u], WO[2 * u + 1]); asm volatile("" : "+v"(WO[2 * u]), "+v"(WO[2 * u + 1])); }
    TS(6);
    WG_BARRIER();
    TS(7);
    const u32x4 o0 = lds_ld<u32x4>(L, L_OB + rg * 32), o1 = lds_ld<u32x4>(L, L_OB + rg * 32 + 16);
#pragma unroll
    for (int u = 0; u < 8; ++u) gemv_pair(WO[2 * u], WO[2 * u + 1], u < 4 ? o0[u & 3] : o1[u & 3], aa);
#pragma unroll
    for (int v = 0; v < 8; ++v) aa[v] = fmaf(aa[v], asf(misc[1]), af[v]);
    const float y0 = swap16_add(aa[0], aa[2]), y1 = swap16_add(aa[1], aa[3]), y2 = swap16_add(aa[4], aa[6]), y3 = swap16_add(aa[5], aa[7]);
    const float z0 = swap32_add(y0, y2), z1 = swap32_add(y1, y3);
    if (c < 14) st_unit(rw, WS_H2 + (((b * 56 + 14 * w4 + c) * 64 + kvh * 8 + s) * 4 + rg) * 16, asu(z0), asu(z1), misc[0]);
    TS(8);
    TS_FLUSH();
    return;
  }

  unsigned tag = 0;
  if (wave < 2) {
    const int lgh = xl + 8 * (r >> 3);
    if (wave == 0 || lgh < 28) {
      const int lg = QMAP ? 8 * xl + (r >> 2) : 8 * (r >> 2) + ((xl + (r >> 2)) & 7);
      const int cq = 4 * lg + (r & 3), ch = 8 * lgh + (r & 7);
      const rsrc_t rx = mk_rsrc(x, NB * DM * 2);
      if (wave == 0) {
        const rsrc_t rq = mk_rsrc(wq + 16 * cq, ((DM - 1) * NQ + 16) * 2);
#pragma unroll
        for (int i = 0; i < 14; ++i)
          __builtin_amdgcn_raw_ptr_buffer_load_lds(rq, (LAS void*)(L + L_WQ + i * 1024), 16, (lane >> 1) * (NQ * 2) + (lane & 1) * 16, i * 32 * NQ * 2, 0, AUX_NT);
      } else {
        const rsrc_t r1 = mk_rsrc(w1 + 8 * ch, ((DM - 1) * DFF + 8) * 2);
#pragma unroll
        for (int i = 0; i < 7; ++i)
          __builtin_amdgcn_raw_ptr_buffer_load_lds(r1, (LAS void*)(L + L_W1 + i * 1024), 16, lane * (DFF * 2), i * 64 * DFF * 2, 0, AUX_NT);
      }
      u32x4 xf[14];
#pragma unroll
      for (int t = 0; t < 14; ++t) xf[t] = ld16<0>(rx, (lane & 15) * (DM * 2) + (lane >> 4) * 16, t * 64);
      tag = __builtin_amdgcn_raw_buffer_load_b32(rw, WS_EPOCH, 0, AUX_SC1) + 1u;
      WAIT_VM0();
      FENCE();
      TS(1);
      const int n = lane & 15, g = lane >> 4;
      if (wave == 0) {
        const f32x4 a = proj_tile<32>(L, L_WQ, xf, lane);
        if (n < NB) st_unit(rw, WS_HQ + (n * 1024 + cq * 4 + g) * 16, pkbf(a[0], a[1]), pkbf(a[2], a[3]), tag);
      } else {
        f32x4 a = proj_tile<16>(L, L_W1, xf, lane);
#pragma unroll
        for (int v = 0; v < 4; ++v) a[v] = a[v] < 0.f ? 0.f : a[v];
        if (n < NB && g < 2) st_unit(rw, WS_HH + (n * 448 + ch * 2 + g) * 16, pkbf(a[0], a[1]), pkbf(a[2], a[3]), tag);
      }
      TS(2);
    }
    if (wave == 0) {
      u32x4 a0, a1, a2;
      const int oq = WS_HQ + (b * 1024 + kvh * 128 + lane) * 16, oh = WS_HH + (b * 448 + jf * 8 + (lane & 7)) * 16;
      for (unsigned it = 0; it < SPIN_CAP; ++it) {
        asm volatile("" ::: "memory");
        a0 = ld16<AUX_SC1>(rw, oq, 0); a1 = ld16<AUX_SC1>(rw, oq, 1024); a2 = ld16<AUX_SC1>(rw, oh, 0);
        if (__all(unit_ok(a0, tag) && unit_ok(a1, tag) && unit_ok(a2, tag))) break;
      }
      lds_st<u32x2>(L, L_QS + lane * 8, u32x2{a0[0], a0[2]});
      lds_st<u32x2>(L, L_QS + 512 + lane * 8, u32x2{a1[0], a1[2]});
      if (lane < 8) lds_st<u32x2>(L, L_HS + lane * 8, red ? u32x2{a2[0], a2[2]} : u32x2{0u, 0u});
      if (lane == 0) lds_st<unsigned>(L, L_MISC, tag);
      TS(3);
    }
  } else if (wave == 2) {
    const rsrc_t rx = mk_rsrc(x + b * DM, DM * 2), rg = mk_rsrc(gw, DM * 2);
    const u32x4 xv = ld16<0>(rx, lane * 16, 0), gv = ld16<0>(rg, lane * 16, 0);
    float t = 0.f;
#pragma unroll
    for (int k = 0; k < 4; ++k) {
      t += rbf(asf(xv[k] << 16) * asf(gv[k] << 16));
      t += rbf(asf(xv[k] & 0xffff0000u) * asf(gv[k] & 0xffff0000u));
    }
    t += dpp<XOR1>(t); t += dpp<XOR2>(t); t += dpp<ROR4>(t); t += dpp<ROR8>(t);
    t = swap16_add(t, t); t = swap32_add(t, t);
    const float g = rbf(1.f / (1.f + __expf(-rbf(t))));
    if (lane == 0) { lds_st<float>(L, L_MISC + 4, g); lds_st<float>(L, L_MISC + 8, rbf(1.f - g)); }
    if (lane < 4) lds_st<unsigned>(L, L_FLAG + lane * 4, 0u);
  } else if (PRETOUCH) {
    const rsrc_t ro = mk_rsrc(wo + (size_t)(2 * kvh * DH + 32 * s) * DM, (DH + 32) * DM * 2);
    const rsrc_t r2 = mk_rsrc(w2 + (size_t)(32 * jf) * DM, red ? 32 * DM * 2 : 0);
    unsigned t0 = __builtin_amdgcn_raw_buffer_load_b32(rv, lane == 0 ? 0 : 0x40000000, KV_SOFF(0, 0), AUX_NT);
    unsigned t1 = __builtin_amdgcn_raw_buffer_load_b32(ro, lane < 2 ? lane * DH * DM * 2 : 0x40000000, 0, AUX_NT);
    unsigned t2 = __builtin_amdgcn_raw_buffer_load_b32(r2, lane == 0 ? 0 : 0x40000000, 0, AUX_NT);
    unsigned t3 = __builtin_amdgcn_raw_buffer_load_b32(rw, lane == 0 ? WS_H1 + (grp * 8 + s) * 4096 : lane == 1 ? WS_H2 + (b * 56 + jf) * 4096 : 0x40000000, 0, AUX_SC1);
    asm volatile("" :: "v"(t0), "v"(t1), "v"(t2), "v"(t3));
  }
  TS(4);
  WG_BARRIER();
  TS(5);
  {
    u32x4 K[16], V[16];
#pragma unroll
    for (int i = 0; i < 16; ++i) { K[i] = ld16<AUX_NT>(rk, lane * 16, KV_SOFF(1, i)); FENCE(); }
    const u32x4 q0 = lds_ld<u32x4>(L, L_QS + (lane & 31) * 16), q1 = lds_ld<u32x4>(L, L_QS + 512 + (lane & 31) * 16);
    tag = lds_ld<unsigned>(L, L_MISC);
    float sc[32];
#pragma unroll
    for (int i = 0; i < 8; ++i) {
      V[i] = ld16<AUX_NT>(rv, lane * 16, KV_SOFF(1, i)); FENCE();
      qk_dots(K[2 * i], q0, q1, sc[4 * i], sc[4 * i + 1]);
      qk_dots(K[2 * i + 1], q0, q1, sc[4 * i + 2], sc[4 * i + 3]);
      FENCE();
    }
#pragma unroll
    for (int i = 8; i < 12; ++i) { V[i] = ld16<AUX_NT>(rv, lane * 16, KV_SOFF(1, i)); FENCE(); }
    TS(6);
    u32x4 pp[4];
    softmax_wave(sc, L, wave, lane, pp);
    float acc[16];
#pragma unroll
    for (int v = 0; v < 16; ++v) acc[v] = 0.f;
    FENCE();
#pragma unroll
    for (int u = 0; u < 2; ++u) {
      V[12 + 2 * u] = ld16<AUX_NT>(rv, lane * 16, KV_SOFF(1, 12 + 2 * u)); FENCE();
      V[13 + 2 * u] = ld16<AUX_NT>(rv, lane * 16, KV_SOFF(1, 13 + 2 * u)); FENCE();
      if (u == 0) { pv_pair(V[0], V[1], pp[0][0], pp[2][0], acc); FENCE(); }
    }
    TS(7);
    WG_BARRIER();
    TS(8);
    const int hd = wave >> 1;
    float wt[8], M = -INFINITY, Ls = 0.f;
    f32x2 ml[8];
#pragma unroll
    for (int w = 0; w < 8; ++w) { ml[w] = lds_ld<f32x2>(L, L_ML + w * 16 + hd * 8); M = fmaxf(M, ml[w][0]); }
#pragma unroll
    for (int w = 0; w < 8; ++w) { wt[w] = __builtin_amdgcn_exp2f(ml[w][0] - (M == -INFINITY ? 0.f : M)); Ls = fmaf(wt[w], ml[w][1], Ls); }
    const int pf = L_PART + wave * 512 + lane * 8;
    f32x2 sum = {0.f, 0.f};
#pragma unroll
    for (int w = 4; w < 8; ++w) { const f32x2 t = lds_ld<f32x2>(L, pf + w * 2048); sum[0] = fmaf(wt[w], t[0], sum[0]); sum[1] = fmaf(wt[w], t[1], sum[1]); }
    FENCE();
#pragma unroll
    for (int u = 1; u < 8; ++u) { pv_pair(V[2 * u], V[2 * u + 1], pp[u >> 2][u & 3], pp[2 + (u >> 2)][u & 3], acc); FENCE(); }
    TS(9);
    store_part(acc, L, wave, lane);
    asm volatile("s_waitcnt lgkmcnt(0)" ::: "memory");
    if (lane == 0) lds_st<unsigned>(L, L_FLAG + wave * 4, 1u);
    for (unsigned it = 0; it < SPIN_CAP; ++it) {
      asm volatile("" ::: "memory");
      const u32x4 f = lds_ld<u32x4>(L, L_FLAG);
      if (__builtin_amdgcn_readfirstlane(f[0] & f[1] & f[2] & f[3])) break;
    }
    asm volatile("" ::: "memory");
#pragma unroll
    for (int w = 0; w < 4; ++w) { const f32x2 t = lds_ld<f32x2>(L, pf + w * 2048); sum[0] = fmaf(wt[w], t[0], sum[0]); sum[1] = fmaf(wt[w], t[1], sum[1]); }
    const int dst = (wave & 1) * 4 + (lane >> 4);
    st_unit(rw, WS_H1 + (((grp * 8 + dst) * 8 + s) * 32 + hd * 16 + (lane & 15)) * 16, asu(sum[0]), asu(sum[1]), tag);
    if (!(wave & 1) && lane == 0) st_unit(rw, WS_H1S + (grp * 16 + s * 2 + hd) * 16, asu(M), asu(Ls), tag);
    TS(10);
  }
  if (wave == 0) {
    u32x4 a[4], st;
    const int oa = WS_H1 + (grp * 8 + s) * 4096 + lane * 16, os = WS_H1S + (grp * 16 + (((lane & 15) + (lane >> 4)) & 15)) * 16;
    for (unsigned it = 0; it < SPIN_CAP; ++it) {
      asm volatile("" ::: "memory");
      st = ld16<AUX_SC1>(rw, os, 0);
#pragma unroll
      for (int i = 0; i < 4; ++i) a[i] = ld16<AUX_SC1>(rw, oa, i * 1024);
      if (__all(unit_ok(st, tag) && unit_ok(a[0], tag) && unit_ok(a[1], tag) && unit_ok(a[2], tag) && unit_ok(a[3], tag))) break;
    }
    TS(11);
    const float Ms = asf(st[0]);
    float Mg = fmaxf(Ms, dpp<XOR2>(Ms)); Mg = fmaxf(Mg, dpp<ROR4>(Mg)); Mg = fmaxf(Mg, dpp<ROR8>(Mg));
    const float e = __builtin_amdgcn_exp2f(Ms - Mg);
    float Lg = e * asf(st[2]);
    Lg += dpp<XOR2>(Lg); Lg += dpp<ROR4>(Lg); Lg += dpp<ROR8>(Lg);
    const float wgt = e * __builtin_amdgcn_rcpf(Lg);
    const float w0 = dpp<BCAST + 0>(wgt), w1_ = dpp<BCAST + 4>(wgt), w2_ = dpp<BCAST + 8>(wgt), w3 = dpp<BCAST + 12>(wgt);
    float o0 = w0 * asf(a[0][0]) + w1_ * asf(a[1][0]) + w2_ * asf(a[2][0]) + w3 * asf(a[3][0]);
    float o1 = w0 * asf(a[0][2]) + w1_ * asf(a[1][2]) + w2_ * asf(a[2][2]) + w3 * asf(a[3][2]);
    o0 = swap32_add(o0, o0); o1 = swap32_add(o1, o1);
    if (lane < 32) lds_st<unsigned>(L, L_OB + lane * 4, pkbf(o0, o1));
  }
  WG_BARRIER();
  TS(12);
  if (wave == 0 && red) {
    u32x4 a[4];
    const int oa = WS_H2 + (b * 56 + jf) * 4096 + lane * 16;
    for (unsigned it = 0; it < SPIN_CAP; ++it) {
      asm volatile("" ::: "memory");
#pragma unroll
      for (int i = 0; i < 4; ++i) a[i] = ld16<AUX_SC1>(rw, oa, i * 1024);
      if (__all(unit_ok(a[0], tag) && unit_ok(a[1], tag) && unit_ok(a[2], tag) && unit_ok(a[3], tag))) break;
    }
    TS(13);
    float o0 = (asf(a[0][0]) + asf(a[1][0])) + (asf(a[2][0]) + asf(a[3][0]));
    float o1 = (asf(a[0][2]) + asf(a[1][2])) + (asf(a[2][2]) + asf(a[3][2]));
    o0 += dpp<ROR4>(o0); o1 += dpp<ROR4>(o1); o0 += dpp<ROR8>(o0); o1 += dpp<ROR8>(o1);
    o0 = swap16_add(o0, o0); o1 = swap16_add(o1, o1); o0 = swap32_add(o0, o0); o1 = swap32_add(o1, o1);
    if (lane < 4) *(unsigned*)(out + b * DM + 8 * jf + 2 * lane) = pkbf(o0, o1);
    if (bid == 0 && lane == 0) __builtin_amdgcn_raw_buffer_store_b32(tag, rw, WS_EPOCH, 0, AUX_SC1);
    TS(14);
  }
  TS_FLUSH();
  if (bid == 0 && wave == 0) probe_tail(ws, tsv[0], lane);
#endif
}

extern "C" void kernel_launch(void* const* d_in, const int* in_sizes, int n_in, void* d_out, int out_size, void* d_ws,
                              size_t ws_size, hipStream_t stream) {
  typedef const unsigned short* P;
  fused_decode<<<256, 512, 0, stream>>>((P)d_in[0], (P)d_in[1], (P)d_in[2], (P)d_in[3], (P)d_in[4], (P)d_in[6], (P)d_in[7],
                                        (P)d_in[8], (unsigned short*)d_out, (char*)d_ws);
}
```

```cpp
#include <hip/hip_runtime.h>
#include <stdint.h>

typedef unsigned u32x4 __attribute__((ext_vector_type(4)));
typedef unsigned u32x2 __attribute__((ext_vector_type(2)));
typedef float f32x4 __attribute__((ext_vector_type(4)));
typedef float f32x2 __attribute__((ext_vector_type(2)));
typedef short s16x4 __attribute__((ext_vector_type(4)));
typedef __bf16 bf16x2 __attribute__((ext_vector_type(2)));
typedef __bf16 bf16x8 __attribute__((ext_vector_type(8)));
#define LAS __attribute__((address_space(3)))
typedef __amdgpu_buffer_rsrc_t rsrc_t;

constexpr int NB = 4, DM = 448, HKV = 8, SK = 8192, DH = 256, WIN = 2048, DFF = 1792, NQ = 4096;

#define MAPX 1
#define GKB 128
#define ROTM 1
#define QMAP 0
#define PRETOUCH 1

constexpr int WS_EPOCH = 0;
constexpr int WS_HQ = 4096;
constexpr int WS_HH = WS_HQ + NB * 1024 * 16;
constexpr int WS_H1S = WS_HH + NB * 448 * 16;
constexpr int WS_H1 = WS_H1S + NB * HKV * 16 * 16;
constexpr int WS_H2 = WS_H1 + NB * HKV * 64 * 32 * 16;
constexpr int WS_END = WS_H2 + NB * 56 * 64 * 64;

constexpr int L_WQ = 0;
constexpr int L_W1 = L_WQ + 14336;
constexpr int L_PART = L_W1 + 7168;
constexpr int L_QS = L_PART + 16384;
constexpr int L_HS = L_QS + 1024;
constexpr int L_OB = L_HS + 64;
constexpr int L_PB = L_OB + 128;
constexpr int L_ML = L_PB + 1024;
constexpr int L_FLAG = L_ML + 128;
constexpr int L_MISC = L_FLAG + 16;
constexpr int L_END = L_MISC + 16;

constexpr int AUX_NT = 2, AUX_SC1 = 16;
constexpr unsigned SPIN_CAP = 1u << 18;
constexpr float LOG2E = 1.4426950408889634f;

#if defined(__HIP_DEVICE_COMPILE__)
__device__ __forceinline__ rsrc_t mk_rsrc(const void* p, unsigned bytes) {
  return __builtin_amdgcn_make_buffer_rsrc((void*)p, 0, bytes, 0x00020000);
}
template <int AUX> __device__ __forceinline__ u32x4 ld16(rsrc_t r, int voff, int soff) {
  return __builtin_amdgcn_raw_buffer_load_b128(r, voff, soff, AUX);
}
__device__ __forceinline__ void st_unit(rsrc_t r, int voff, unsigned a, unsigned b, unsigned tag) {
  __builtin_amdgcn_raw_buffer_store_b128(u32x4{a, tag, b, tag}, r, voff, 0, AUX_SC1);
}
__device__ __forceinline__ float asf(unsigned u) { return __builtin_bit_cast(float, u); }
__device__ __forceinline__ unsigned asu(float f) { return __builtin_bit_cast(unsigned, f); }
__device__ __forceinline__ unsigned pkbf(float lo, float hi) {
  return __builtin_bit_cast(unsigned, __builtin_convertvector((f32x2{lo, hi}), bf16x2));
}
__device__ __forceinline__ float rbf(float f) { return asf(pkbf(f, f) << 16); }
__device__ __forceinline__ float dot2(unsigned a, unsigned b, float acc) {
  return __builtin_amdgcn_fdot2_f32_bf16(__builtin_bit_cast(bf16x2, a), __builtin_bit_cast(bf16x2, b), acc, false);
}
template <int CTRL> __device__ __forceinline__ float dpp(float x) {
  return asf(__builtin_amdgcn_mov_dpp(asu(x), CTRL, 0xf, 0xf, true));
}
constexpr int XOR1 = 0xB1, XOR2 = 0x4E, XOR7 = 0x141, ROR8 = 0x128, ROR4 = 0x124, BCAST = 0x150;
__device__ __forceinline__ float swap16_add(float a, float b) {
  auto s = __builtin_amdgcn_permlane16_swap(asu(a), asu(b), false, false);
  return asf(s[0]) + asf(s[1]);
}
__device__ __forceinline__ float swap32_add(float a, float b) {
  auto s = __builtin_amdgcn_permlane32_swap(asu(a), asu(b), false, false);
  return asf(s[0]) + asf(s[1]);
}
__device__ __forceinline__ float swap16_max(float a) {
  auto s = __builtin_amdgcn_permlane16_swap(asu(a), asu(a), false, false);
  return fmaxf(asf(s[0]), asf(s[1]));
}
__device__ __forceinline__ float swap32_max(float a) {
  auto s = __builtin_amdgcn_permlane32_swap(asu(a), asu(a), false, false);
  return fmaxf(asf(s[0]), asf(s[1]));
}
#define FENCE() __builtin_amdgcn_sched_barrier(0)
#define WAIT_VM0() asm volatile("s_waitcnt vmcnt(0)" ::: "memory")
#define WG_BARRIER() do { asm volatile("s_waitcnt lgkmcnt(0)\n\ts_barrier" ::: "memory"); } while (0)
template <typename T> __device__ __forceinline__ T lds_ld(LAS char* L, int off) { return *(LAS T*)(L + off); }
template <typename T> __device__ __forceinline__ void lds_st(LAS char* L, int off, T v) { *(LAS T*)(L + off) = v; }

#define TS_DECL do {} while (0)
#define TS(i) do {} while (0)
#define TS_FLUSH() do {} while (0)

template <int ROWB> __device__ __forceinline__ f32x4 proj_tile(LAS char* L, int lbase, const u32x4 (&xf)[14], int lane) {
  const int g = lane >> 4, q = (lane & 15) >> 2, p = lane & 3;
  const int a0 = lbase + (8 * g + q) * ROWB + 8 * (ROWB == 32 ? p : (p & 1));
  f32x4 acc = {0.f, 0.f, 0.f, 0.f};
#pragma unroll
  for (int t = 0; t < 14; ++t) {
    s16x4 lo = __builtin_amdgcn_ds_read_tr16_b64_v4i16((LAS s16x4*)(L + a0 + t * 32 * ROWB));
    s16x4 hi = __builtin_amdgcn_ds_read_tr16_b64_v4i16((LAS s16x4*)(L + a0 + t * 32 * ROWB + 4 * ROWB));
    u32x2 l2 = __builtin_bit_cast(u32x2, lo), h2 = __builtin_bit_cast(u32x2, hi);
    u32x4 w = {l2[0], l2[1], h2[0], h2[1]};
    acc = __builtin_amdgcn_mfma_f32_16x16x32_bf16(__builtin_bit_cast(bf16x8, w), __builtin_bit_cast(bf16x8, xf[t]), acc, 0, 0, 0);
  }
  return acc;
}

__device__ __forceinline__ void qk_dots(const u32x4& k, const u32x4& q0, const u32x4& q1, float& d0, float& d1) {
  float a = 0.f, b = 0.f;
#pragma unroll
  for (int j = 0; j < 4; ++j) { a = dot2(k[j], q0[j], a); b = dot2(k[j], q1[j], b); }
  d0 = a; d1 = b;
}

__device__ __forceinline__ void softmax_wave(float (&x)[32], LAS char* L, int wave, int lane, u32x4 (&pp)[4]) {
#pragma unroll
  for (int v = 0; v < 16; ++v) x[v] = swap16_add(x[v], x[v + 16]);
#define TR_STEP(H, CTRL, BIT) _Pragma("unroll") for (int v = 0; v < (H); ++v) { \
    const float t1 = x[v] + dpp<CTRL>(x[v]), t2 = x[v + (H)] + dpp<CTRL>(x[v + (H)]); x[v] = (lane & (BIT)) ? t2 : t1; }
  TR_STEP(8, ROR8, 8) TR_STEP(4, XOR7, 4) TR_STEP(2, XOR2, 2) TR_STEP(1, XOR1, 1)
#undef TR_STEP
  const float t = rbf(x[0]) * (0.0625f * LOG2E);
  float m = fmaxf(t, dpp<XOR2>(t));
  m = fmaxf(m, dpp<ROR4>(m)); m = fmaxf(m, dpp<ROR8>(m)); m = swap32_max(swap16_max(m));
  const float p = __builtin_amdgcn_exp2f(t - (m == -INFINITY ? 0.f : m));
  const float po = dpp<XOR2>(p);
  float l = p + po;
  l += dpp<ROR4>(l); l += dpp<ROR8>(l); l = swap16_add(l, l); l = swap32_add(l, l);
  const int pb = L_PB + wave * 128;
  if (!(lane & 2)) lds_st<unsigned>(L, pb + (((lane >> 5) * 2 + (lane & 1)) * 8 + ((lane & 31) >> 2)) * 4, pkbf(p, po));
  if (lane < 2) lds_st<f32x2>(L, L_ML + wave * 16 + lane * 8, f32x2{m, l});
  asm volatile("s_waitcnt lgkmcnt(0)" ::: "memory");
#pragma unroll
  for (int j = 0; j < 4; ++j) pp[j] = lds_ld<u32x4>(L, pb + (lane >> 5) * 64 + j * 16);
}

__device__ __forceinline__ void pv_pair(const u32x4& v0, const u32x4& v1, unsigned p0, unsigned p1, float (&acc)[16]) {
#pragma unroll
  for (int k = 0; k < 4; ++k) {
    const unsigned lo = __builtin_amdgcn_perm(v1[k], v0[k], 0x05040100u), hi = __builtin_amdgcn_perm(v1[k], v0[k], 0x07060302u);
    acc[2 * k] = dot2(lo, p0, acc[2 * k]); acc[2 * k + 1] = dot2(hi, p0, acc[2 * k + 1]);
    acc[8 + 2 * k] = dot2(lo, p1, acc[8 + 2 * k]); acc[9 + 2 * k] = dot2(hi, p1, acc[9 + 2 * k]);
  }
}
__device__ __forceinline__ void store_part(float (&acc)[16], LAS char* L, int wave, int lane) {
  f32x4 a, b;
#pragma unroll
  for (int v = 0; v < 4; ++v) { a[v] = swap32_add(acc[v], acc[v + 8]); b[v] = swap32_add(acc[v + 4], acc[v + 12]); }
  lds_st<f32x4>(L, L_PART + wave * 2048 + lane * 32, a);
  lds_st<f32x4>(L, L_PART + wave * 2048 + lane * 32 + 16, b);
}
__device__ __forceinline__ void pair_rows(u32x4& r0, u32x4& r1) {
#pragma unroll
  for (int k = 0; k < 4; ++k) {
    const unsigned lo = __builtin_amdgcn_perm(r1[k], r0[k], 0x05040100u), hi = __builtin_amdgcn_perm(r1[k], r0[k], 0x07060302u);
    r0[k] = lo; r1[k] = hi;
  }
}
__device__ __forceinline__ void gemv_pair(const u32x4& lo, const u32x4& hi, unsigned a, float (&acc)[8]) {
#pragma unroll
  for (int k = 0; k < 4; ++k) { acc[2 * k] = dot2(lo[k], a, acc[2 * k]); acc[2 * k + 1] = dot2(hi[k], a, acc[2 * k + 1]); }
}
__device__ __forceinline__ bool unit_ok(const u32x4& u, unsigned tag) { return u[1] == tag && u[3] == tag; }
__device__ __forceinline__ int kb_of(int u, int s) { return (u / GKB) * (8 * GKB) + s * GKB + (u % GKB); }
#endif

__global__ void __launch_bounds__(512, 2)
fused_decode(const unsigned short* __restrict__ x, const unsigned short* __restrict__ kc, const unsigned short* __restrict__ vc,
             const unsigned short* __restrict__ wq, const unsigned short* __restrict__ wo, const unsigned short* __restrict__ w1,
             const unsigned short* __restrict__ w2, const unsigned short* __restrict__ gw, unsigned short* __restrict__ out,
             char* __restrict__ ws) {
#if defined(__HIP_DEVICE_COMPILE__)
  __shared__ __attribute__((aligned(16))) char smem[L_END];
  LAS char* L = (LAS char*)smem;
  const int lane = threadIdx.x & 63, wave = __builtin_amdgcn_readfirstlane(threadIdx.x >> 6);
  const int bid = blockIdx.x, xl = bid & 7, r = bid >> 3, b = r >> 3;
  const int kvh = MAPX ? xl : (r & 7), s = MAPX ? (r & 7) : xl;
  const int grp = b * HKV + kvh;
  const bool red = s < 7;
  const int jf = red ? kvh * 7 + s : 0;
  const size_t hb = ((size_t)grp * SK + (SK - WIN)) * DH;
  const rsrc_t rk = mk_rsrc(kc + hb, WIN * DH * 2), rv = mk_rsrc(vc + hb, WIN * DH * 2);
  const rsrc_t rw = mk_rsrc(ws, WS_END);
  const int rot = ROTM == 0 ? 0 : ((bid * 37) & 31) * 4;
  const int w4 = wave & 3;
#define KV_SOFF(G, i) (kb_of(((G) * 64 + 4 * (i) + w4 + rot) & 127, s) * 1024)
  TS_DECL;
  TS(0);

  if (wave >= 4) {
    u32x4 K[16], V[16];
#pragma unroll
    for (int i = 0; i < 16; ++i) { K[i] = ld16<AUX_NT>(rk, lane * 16, KV_SOFF(0, i)); FENCE(); }
#pragma unroll
    for (int i = 0; i < 16; ++i) { V[i] = ld16<AUX_NT>(rv, lane * 16, KV_SOFF(0, i)); FENCE(); }
    TS(1);
    WG_BARRIER();
    TS(2);
    const u32x4 q0 = lds_ld<u32x4>(L, L_QS + (lane & 31) * 16), q1 = lds_ld<u32x4>(L, L_QS + 512 + (lane & 31) * 16);
    float sc[32];
#pragma unroll
    for (int i = 0; i < 16; ++i) qk_dots(K[i], q0, q1, sc[2 * i], sc[2 * i + 1]);
    u32x4 pp[4];
    softmax_wave(sc, L, wave, lane, pp);
    float acc[16];
#pragma unroll
    for (int v = 0; v < 16; ++v) acc[v] = 0.f;
#pragma unroll
    for (int u = 0; u < 8; ++u) pv_pair(V[2 * u], V[2 * u + 1], pp[u >> 2][u & 3], pp[2 + (u >> 2)][u & 3], acc);
    store_part(acc, L, wave, lane);
    const int rg = lane >> 4, c = lane & 15;
    const rsrc_t ro = mk_rsrc(wo + (size_t)(2 * kvh * DH + 32 * s) * DM, (DH + 32) * DM * 2);
    const rsrc_t r2 = mk_rsrc(w2 + (size_t)(32 * jf) * DM, red ? 32 * DM * 2 : 0);
    const int colb = c < 14 ? 224 * w4 + 16 * c : 0x40000000;
    const int vo = ((rg >> 1) * DH + (rg & 1) * 16) * (DM * 2) + colb, v2 = rg * 8 * (DM * 2) + colb;
    const u32x4 misc = lds_ld<u32x4>(L, L_MISC), hp = lds_ld<u32x4>(L, L_HS + rg * 16);
    TS(3);
    WG_BARRIER();
    TS(4);
    u32x4 W2[8], WO[16];
#pragma unroll
    for (int i = 0; i < 8; ++i) { W2[i] = ld16<AUX_NT>(r2, v2, i * DM * 2); FENCE(); }
#pragma unroll
    for (int i = 0; i < 16; ++i) { WO[i] = ld16<AUX_NT>(ro, vo, i * DM * 2); FENCE(); }
    TS(5);
    float af[8], aa[8];
#pragma unroll
    for (int v = 0; v < 8; ++v) { af[v] = 0.f; aa[v] = 0.f; }
#pragma unroll
    for (int u = 0; u < 4; ++u) { pair_rows(W2[2 * u], W2[2 * u + 1]); gemv_pair(W2[2 * u], W2[2 * u + 1], hp[u], af); }
#pragma unroll
    for (int v = 0; v < 8; ++v) af[v] *= asf(misc[2]);
#pragma unroll
    for (int u = 0; u < 8; ++u) { pair_rows(WO[2 * u], WO[2 * u + 1]); asm volatile("" : "+v"(WO[2 * u]), "+v"(WO[2 * u + 1])); }
    TS(6);
    WG_BARRIER();
    TS(7);
    const u32x4 o0 = lds_ld<u32x4>(L, L_OB + rg * 32), o1 = lds_ld<u32x4>(L, L_OB + rg * 32 + 16);
#pragma unroll
    for (int u = 0; u < 8; ++u) gemv_pair(WO[2 * u], WO[2 * u + 1], u < 4 ? o0[u & 3] : o1[u & 3], aa);
#pragma unroll
    for (int v = 0; v < 8; ++v) aa[v] = fmaf(aa[v], asf(misc[1]), af[v]);
    const float y0 = swap16_add(aa[0], aa[2]), y1 = swap16_add(aa[1], aa[3]), y2 = swap16_add(aa[4], aa[6]), y3 = swap16_add(aa[5], aa[7]);
    const float z0 = swap32_add(y0, y2), z1 = swap32_add(y1, y3);
    if (c < 14) st_unit(rw, WS_H2 + (((b * 56 + 14 * w4 + c) * 64 + kvh * 8 + s) * 4 + rg) * 16, asu(z0), asu(z1), misc[0]);
    TS(8);
    TS_FLUSH();
    return;
  }

  unsigned tag = 0;
  if (wave < 2) {
    const int lgh = xl + 8 * (r >> 3);
    if (wave == 0 || lgh < 28) {
      const int lg = QMAP ? 8 * xl + (r >> 2) : 8 * (r >> 2) + ((xl + (r >> 2)) & 7);
      const int cq = 4 * lg + (r & 3), ch = 8 * lgh + (r & 7);
      const rsrc_t rx = mk_rsrc(x, NB * DM * 2);
      if (wave == 0) {
        const rsrc_t rq = mk_rsrc(wq + 16 * cq, ((DM - 1) * NQ + 16) * 2);
#pragma unroll
        for (int i = 0; i < 14; ++i)
          __builtin_amdgcn_raw_ptr_buffer_load_lds(rq, (LAS void*)(L + L_WQ + i * 1024), 16, (lane >> 1) * (NQ * 2) + (lane & 1) * 16, i * 32 * NQ * 2, 0, AUX_NT);
      } else {
        const rsrc_t r1 = mk_rsrc(w1 + 8 * ch, ((DM - 1) * DFF + 8) * 2);
#pragma unroll
        for (int i = 0; i < 7; ++i)
          __builtin_amdgcn_raw_ptr_buffer_load_lds(r1, (LAS void*)(L + L_W1 + i * 1024), 16, lane * (DFF * 2), i * 64 * DFF * 2, 0, AUX_NT);
      }
      u32x4 xf[14];
#pragma unroll
      for (int t = 0; t < 14; ++t) xf[t] = ld16<0>(rx, (lane & 15) * (DM * 2) + (lane >> 4) * 16, t * 64);
      tag = __builtin_amdgcn_raw_buffer_load_b32(rw, WS_EPOCH, 0, AUX_SC1) + 1u;
      WAIT_VM0();
      FENCE();
      TS(1);
      const int n = lane & 15, g = lane >> 4;
      if (wave == 0) {
        const f32x4 a = proj_tile<32>(L, L_WQ, xf, lane);
        if (n < NB) st_unit(rw, WS_HQ + (n * 1024 + cq * 4 + g) * 16, pkbf(a[0], a[1]), pkbf(a[2], a[3]), tag);
      } else {
        f32x4 a = proj_tile<16>(L, L_W1, xf, lane);
#pragma unroll
        for (int v = 0; v < 4; ++v) a[v] = a[v] < 0.f ? 0.f : a[v];
        if (n < NB && g < 2) st_unit(rw, WS_HH + (n * 448 + ch * 2 + g) * 16, pkbf(a[0], a[1]), pkbf(a[2], a[3]), tag);
      }
      TS(2);
    }
    if (wave == 0) {
      u32x4 a0, a1, a2;
      const int oq = WS_HQ + (b * 1024 + kvh * 128 + lane) * 16, oh = WS_HH + (b * 448 + jf * 8 + (lane & 7)) * 16;
      for (unsigned it = 0; it < SPIN_CAP; ++it) {
        asm volatile("" ::: "memory");
        a0 = ld16<AUX_SC1>(rw, oq, 0); a1 = ld16<AUX_SC1>(rw, oq, 1024); a2 = ld16<AUX_SC1>(rw, oh, 0);
        if (__all(unit_ok(a0, tag) && unit_ok(a1, tag) && unit_ok(a2, tag))) break;
      }
      lds_st<u32x2>(L, L_QS + lane * 8, u32x2{a0[0], a0[2]});
      lds_st<u32x2>(L, L_QS + 512 + lane * 8, u32x2{a1[0], a1[2]});
      if (lane < 8) lds_st<u32x2>(L, L_HS + lane * 8, red ? u32x2{a2[0], a2[2]} : u32x2{0u, 0u});
      if (lane == 0) lds_st<unsigned>(L, L_MISC, tag);
      TS(3);
    }
  } else if (wave == 2) {
    const rsrc_t rx = mk_rsrc(x + b * DM, DM * 2), rg = mk_rsrc(gw, DM * 2);
    const u32x4 xv = ld16<0>(rx, lane * 16, 0), gv = ld16<0>(rg, lane * 16, 0);
    float t = 0.f;
#pragma unroll
    for (int k = 0; k < 4; ++k) {
      t += rbf(asf(xv[k] << 16) * asf(gv[k] << 16));
      t += rbf(asf(xv[k] & 0xffff0000u) * asf(gv[k] & 0xffff0000u));
    }
    t += dpp<XOR1>(t); t += dpp<XOR2>(t); t += dpp<ROR4>(t); t += dpp<ROR8>(t);
    t = swap16_add(t, t); t = swap32_add(t, t);
    const float g = rbf(1.f / (1.f + __expf(-rbf(t))));
    if (lane == 0) { lds_st<float>(L, L_MISC + 4, g); lds_st<float>(L, L_MISC + 8, rbf(1.f - g)); }
    if (lane < 4) lds_st<unsigned>(L, L_FLAG + lane * 4, 0u);
  } else if (PRETOUCH) {
    const rsrc_t ro = mk_rsrc(wo + (size_t)(2 * kvh * DH + 32 * s) * DM, (DH + 32) * DM * 2);
    const rsrc_t r2 = mk_rsrc(w2 + (size_t)(32 * jf) * DM, red ? 32 * DM * 2 : 0);
    unsigned t0 = __builtin_amdgcn_raw_buffer_load_b32(rv, lane == 0 ? 0 : 0x40000000, KV_SOFF(0, 0), AUX_NT);
    unsigned t1 = __builtin_amdgcn_raw_buffer_load_b32(ro, lane < 2 ? lane * DH * DM * 2 : 0x40000000, 0, AUX_NT);
    unsigned t2 = __builtin_amdgcn_raw_buffer_load_b32(r2, lane == 0 ? 0 : 0x40000000, 0, AUX_NT);
    unsigned t3 = __builtin_amdgcn_raw_buffer_load_b32(rw, lane == 0 ? WS_H1 + (grp * 8 + s) * 4096 : lane == 1 ? WS_H2 + (b * 56 + jf) * 4096 : 0x40000000, 0, AUX_SC1);
    asm volatile("" :: "v"(t0), "v"(t1), "v"(t2), "v"(t3));
  }
  TS(4);
  WG_BARRIER();
  TS(5);
  {
    u32x4 K[16], V[16];
#pragma unroll
    for (int i = 0; i < 16; ++i) { K[i] = ld16<AUX_NT>(rk, lane * 16, KV_SOFF(1, i)); FENCE(); }
    const u32x4 q0 = lds_ld<u32x4>(L, L_QS + (lane & 31) * 16), q1 = lds_ld<u32x4>(L, L_QS + 512 + (lane & 31) * 16);
    tag = lds_ld<unsigned>(L, L_MISC);
    float sc[32];
#pragma unroll
    for (int i = 0; i < 8; ++i) {
      V[i] = ld16<AUX_NT>(rv, lane * 16, KV_SOFF(1, i)); FENCE();
      qk_dots(K[2 * i], q0, q1, sc[4 * i], sc[4 * i + 1]);
      qk_dots(K[2 * i + 1], q0, q1, sc[4 * i + 2], sc[4 * i + 3]);
      FENCE();
    }
#pragma unroll
    for (int i = 8; i < 12; ++i) { V[i] = ld16<AUX_NT>(rv, lane * 16, KV_SOFF(1, i)); FENCE(); }
    TS(6);
    u32x4 pp[4];
    softmax_wave(sc, L, wave, lane, pp);
    float acc[16];
#pragma unroll
    for (int v = 0; v < 16; ++v) acc[v] = 0.f;
    FENCE();
#pragma unroll
    for (int u = 0; u < 2; ++u) {
      V[12 + 2 * u] = ld16<AUX_NT>(rv, lane * 16, KV_SOFF(1, 12 + 2 * u)); FENCE();
      V[13 + 2 * u] = ld16<AUX_NT>(rv, lane * 16, KV_SOFF(1, 13 + 2 * u)); FENCE();
      if (u == 0) { pv_pair(V[0], V[1], pp[0][0], pp[2][0], acc); FENCE(); }
    }
    TS(7);
    WG_BARRIER();
    TS(8);
    const int hd = wave >> 1;
    float wt[8], M = -INFINITY, Ls = 0.f;
    f32x2 ml[8];
#pragma unroll
    for (int w = 0; w < 8; ++w) { ml[w] = lds_ld<f32x2>(L, L_ML + w * 16 + hd * 8); M = fmaxf(M, ml[w][0]); }
#pragma unroll
    for (int w = 0; w < 8; ++w) { wt[w] = __builtin_amdgcn_exp2f(ml[w][0] - (M == -INFINITY ? 0.f : M)); Ls = fmaf(wt[w], ml[w][1], Ls); }
    const int pf = L_PART + wave * 512 + lane * 8;
    f32x2 sum = {0.f, 0.f};
#pragma unroll
    for (int w = 4; w < 8; ++w) { const f32x2 t = lds_ld<f32x2>(L, pf + w * 2048); sum[0] = fmaf(wt[w], t[0], sum[0]); sum[1] = fmaf(wt[w], t[1], sum[1]); }
    FENCE();
#pragma unroll
    for (int u = 1; u < 8; ++u) { pv_pair(V[2 * u], V[2 * u + 1], pp[u >> 2][u & 3], pp[2 + (u >> 2)][u & 3], acc); FENCE(); }
    TS(9);
    store_part(acc, L, wave, lane);
    asm volatile("s_waitcnt lgkmcnt(0)" ::: "memory");
    if (lane == 0) lds_st<unsigned>(L, L_FLAG + wave * 4, 1u);
    for (unsigned it = 0; it < SPIN_CAP; ++it) {
      asm volatile("" ::: "memory");
      const u32x4 f = lds_ld<u32x4>(L, L_FLAG);
      if (__builtin_amdgcn_readfirstlane(f[0] & f[1] & f[2] & f[3])) break;
    }
    asm volatile("" ::: "memory");
#pragma unroll
    for (int w = 0; w < 4; ++w) { const f32x2 t = lds_ld<f32x2>(L, pf + w * 2048); sum[0] = fmaf(wt[w], t[0], sum[0]); sum[1] = fmaf(wt[w], t[1], sum[1]); }
    const int dst = (wave & 1) * 4 + (lane >> 4);
    st_unit(rw, WS_H1 + (((grp * 8 + dst) * 8 + s) * 32 + hd * 16 + (lane & 15)) * 16, asu(sum[0]), asu(sum[1]), tag);
    if (!(wave & 1) && lane == 0) st_unit(rw, WS_H1S + (grp * 16 + s * 2 + hd) * 16, asu(M), asu(Ls), tag);
    TS(10);
  }
  if (wave == 0) {
    u32x4 a[4], st;
    const int oa = WS_H1 + (grp * 8 + s) * 4096 + lane * 16, os = WS_H1S + (grp * 16 + (((lane & 15) + (lane >> 4)) & 15)) * 16;
    for (unsigned it = 0; it < SPIN_CAP; ++it) {
      asm volatile("" ::: "memory");
      st = ld16<AUX_SC1>(rw, os, 0);
#pragma unroll
      for (int i = 0; i < 4; ++i) a[i] = ld16<AUX_SC1>(rw, oa, i * 1024);
      if (__all(unit_ok(st, tag) && unit_ok(a[0], tag) && unit_ok(a[1], tag) && unit_ok(a[2], tag) && unit_ok(a[3], tag))) break;
    }
    TS(11);
    const float Ms = asf(st[0]);
    float Mg = fmaxf(Ms, dpp<XOR2>(Ms)); Mg = fmaxf(Mg, dpp<ROR4>(Mg)); Mg = fmaxf(Mg, dpp<ROR8>(Mg));
    const float e = __builtin_amdgcn_exp2f(Ms - Mg);
    float Lg = e * asf(st[2]);
    Lg += dpp<XOR2>(Lg); Lg += dpp<ROR4>(Lg); Lg += dpp<ROR8>(Lg);
    const float wgt = e * __builtin_amdgcn_rcpf(Lg);
    const float w0 = dpp<BCAST + 0>(wgt), w1_ = dpp<BCAST + 4>(wgt), w2_ = dpp<BCAST + 8>(wgt), w3 = dpp<BCAST + 12>(wgt);
    float o0 = w0 * asf(a[0][0]) + w1_ * asf(a[1][0]) + w2_ * asf(a[2][0]) + w3 * asf(a[3][0]);
    float o1 = w0 * asf(a[0][2]) + w1_ * asf(a[1][2]) + w2_ * asf(a[2][2]) + w3 * asf(a[3][2]);
    o0 = swap32_add(o0, o0); o1 = swap32_add(o1, o1);
    if (lane < 32) lds_st<unsigned>(L, L_OB + lane * 4, pkbf(o0, o1));
  }
  WG_BARRIER();
  TS(12);
  if (wave == 0 && red) {
    u32x4 a[4];
    const int oa = WS_H2 + (b * 56 + jf) * 4096 + lane * 16;
    for (unsigned it = 0; it < SPIN_CAP; ++it) {
      asm volatile("" ::: "memory");
#pragma unroll
      for (int i = 0; i < 4; ++i) a[i] = ld16<AUX_SC1>(rw, oa, i * 1024);
      if (__all(unit_ok(a[0], tag) && unit_ok(a[1], tag) && unit_ok(a[2], tag) && unit_ok(a[3], tag))) break;
    }
    TS(13);
    float o0 = (asf(a[0][0]) + asf(a[1][0])) + (asf(a[2][0]) + asf(a[3][0]));
    float o1 = (asf(a[0][2]) + asf(a[1][2])) + (asf(a[2][2]) + asf(a[3][2]));
    o0 += dpp<ROR4>(o0); o1 += dpp<ROR4>(o1); o0 += dpp<ROR8>(o0); o1 += dpp<ROR8>(o1);
    o0 = swap16_add(o0, o0); o1 = swap16_add(o1, o1); o0 = swap32_add(o0, o0); o1 = swap32_add(o1, o1);
    if (lane < 4) *(unsigned*)(out + b * DM + 8 * jf + 2 * lane) = pkbf(o0, o1);
    if (bid == 0 && lane == 0) __builtin_amdgcn_raw_buffer_store_b32(tag, rw, WS_EPOCH, 0, AUX_SC1);
    TS(14);
  }
  TS_FLUSH();
#endif
}

extern "C" void kernel_launch(void* const* d_in, const int* in_sizes, int n_in, void* d_out, int out_size, void* d_ws,
                              size_t ws_size, hipStream_t stream) {
  typedef const unsigned short* P;
  fused_decode<<<256, 512, 0, stream>>>((P)d_in[0], (P)d_in[1], (P)d_in[2], (P)d_in[3], (P)d_in[4], (P)d_in[6], (P)d_in[7],
                                        (P)d_in[8], (unsigned short*)d_out, (char*)d_ws);
}
```

```cpp
#include <hip/hip_runtime.h>
#include <stdint.h>

typedef unsigned u32x4 __attribute__((ext_vector_type(4)));
typedef unsigned u32x2 __attribute__((ext_vector_type(2)));
typedef float f32x4 __attribute__((ext_vector_type(4)));
typedef float f32x2 __attribute__((ext_vector_type(2)));
typedef short s16x4 __attribute__((ext_vector_type(4)));
typedef __bf16 bf16x2 __attribute__((ext_vector_type(2)));
typedef __bf16 bf16x8 __attribute__((ext_vector_type(8)));
#define LAS __attribute__((address_space(3)))
typedef __amdgpu_buffer_rsrc_t rsrc_t;

constexpr int NB = 4, DM = 448, HKV = 8, SK = 8192, DH = 256, WIN = 2048, DFF = 1792, NQ = 4096;

#define MAPX 0
#define GKB 128
#define ROTM 2
#define QMAP 0
#define PRETOUCH 1

#define D1 8
#define D2 28
#define D0 36
#define LAG 6
#define PREW 1
#define WSOFF 0
#define H0AUX 16
#define BPRE 0
#define WOE 0
#define PS1 0
#define PS2 0
#define WOPOS 1
#define WAUX 0
#define PAUX 2
constexpr int WS_EPOCH = 0;
constexpr int WS_HQ = 1024;
constexpr int WS_HH = WS_HQ + NB * 4 * 1024 * 16;
constexpr int WS_H1S = WS_HH + NB * 7 * 448 * 16;
constexpr int WS_H1 = WS_H1S + NB * HKV * 16 * 16;
constexpr int WS_H2 = WS_H1 + NB * HKV * 64 * 16 * 16;
constexpr int WS_END = WS_H2 + NB * 28 * 64 * 64;

constexpr int L_WQ = 0;
constexpr int L_W1 = L_WQ + 14336;
constexpr int L_PART = L_W1 + 8192;
constexpr int L_X = L_PART + 16384;
constexpr int L_QS = L_X + 4096;
constexpr int L_HS = L_QS + 1024;
constexpr int L_OB = L_HS + 64;
constexpr int L_PB = L_OB + 128;
constexpr int L_ML = L_PB + 1024;
constexpr int L_FLAG = L_ML + 128;
constexpr int F_PRO = 0, F_STA = 16, F_STB = 32, F_ISS = 48, F_Q = 64, F_VI = 80, F_PA = 96, F_H2 = 112;
constexpr int L_MISC = L_FLAG + 128;
constexpr int L_END = L_MISC + 16;

constexpr int AUX_NT = 2, AUX_SC1 = 16, OOB = 0x40000000;
constexpr unsigned SPIN_CAP = 1u << 18;
constexpr float LOG2E = 1.4426950408889634f;

#if defined(__HIP_DEVICE_COMPILE__)
__device__ __forceinline__ rsrc_t mk_rsrc(const void* p, unsigned bytes) {
  return __builtin_amdgcn_make_buffer_rsrc((void*)p, 0, bytes, 0x00020000);
}
template <int AUX> __device__ __forceinline__ u32x4 ld16(rsrc_t r, int voff, int soff) {
  return __builtin_amdgcn_raw_buffer_load_b128(r, voff, soff, AUX);
}
__device__ __forceinline__ u32x4 ld_unit(rsrc_t r, int voff, int soff) {
  asm volatile("" : "+v"(voff));
  return __builtin_amdgcn_raw_buffer_load_b128(r, voff, soff, AUX_SC1);
}
__device__ __forceinline__ void st_unit(rsrc_t r, int voff, unsigned a, unsigned b, unsigned tag) {
  __builtin_amdgcn_raw_buffer_store_b128(u32x4{a, tag, b, tag}, r, voff, 0, AUX_SC1);
}
template <int AUX> __device__ __forceinline__ void st16(rsrc_t r, int voff, u32x4 v) { __builtin_amdgcn_raw_buffer_store_b128(v, r, voff, 0, AUX); }
__device__ __forceinline__ float asf(unsigned u) { return __builtin_bit_cast(float, u); }
__device__ __forceinline__ unsigned asu(float f) { return __builtin_bit_cast(unsigned, f); }
__device__ __forceinline__ unsigned pkbf(float lo, float hi) {
  return __builtin_bit_cast(unsigned, __builtin_convertvector((f32x2{lo, hi}), bf16x2));
}
__device__ __forceinline__ float rbf(float f) { return asf(pkbf(f, f) << 16); }
__device__ __forceinline__ float dot2(unsigned a, unsigned b, float acc) {
  return __builtin_amdgcn_fdot2_f32_bf16(__builtin_bit_cast(bf16x2, a), __builtin_bit_cast(bf16x2, b), acc, false);
}
template <int CTRL> __device__ __forceinline__ float dpp(float x) {
  return asf(__builtin_amdgcn_mov_dpp(asu(x), CTRL, 0xf, 0xf, true));
}
constexpr int XOR1 = 0xB1, XOR2 = 0x4E, XOR7 = 0x141, ROR8 = 0x128, ROR4 = 0x124, BCAST = 0x150;
__device__ __forceinline__ float swap16_add(float a, float b) {
  auto s = __builtin_amdgcn_permlane16_swap(asu(a), asu(b), false, false);
  return asf(s[0]) + asf(s[1]);
}
__device__ __forceinline__ float swap32_add(float a, float b) {
  auto s = __builtin_amdgcn_permlane32_swap(asu(a), asu(b), false, false);
  return asf(s[0]) + asf(s[1]);
}
__device__ __forceinline__ float swap16_max(float a) {
  auto s = __builtin_amdgcn_permlane16_swap(asu(a), asu(a), false, false);
  return fmaxf(asf(s[0]), asf(s[1]));
}
__device__ __forceinline__ float swap32_max(float a) {
  auto s = __builtin_amdgcn_permlane32_swap(asu(a), asu(a), false, false);
  return fmaxf(asf(s[0]), asf(s[1]));
}
#define FENCE() __builtin_amdgcn_sched_barrier(0)
#define WAIT_VM0() asm volatile("s_waitcnt vmcnt(0)" ::: "memory")
#define WG_BARRIER() do { asm volatile("s_waitcnt lgkmcnt(0)\n\ts_barrier" ::: "memory"); } while (0)
template <typename T> __device__ __forceinline__ T lds_ld(LAS char* L, int off) { return *(LAS T*)(L + off); }
template <typename T> __device__ __forceinline__ void lds_st(LAS char* L, int off, T v) { *(LAS T*)(L + off) = v; }

#define TS_DECL do {} while (0)
#define TS(i) do {} while (0)
#define TS_FLUSH() do {} while (0)

__device__ __forceinline__ u32x2 tr_read(LAS char* L, int off) {
  return __builtin_bit_cast(u32x2, __builtin_amdgcn_ds_read_tr16_b64_v4i16((LAS s16x4*)(L + off)));
}
__device__ __forceinline__ f32x4 proj_step32(LAS char* L, int a, const u32x4& xf, f32x4 acc) {
  const u32x2 lo = tr_read(L, a), hi = tr_read(L, a + 4 * 128);
  const u32x4 w = {lo[0], lo[1], hi[0], hi[1]};
  return __builtin_amdgcn_mfma_f32_16x16x32_bf16(__builtin_bit_cast(bf16x8, w), __builtin_bit_cast(bf16x8, xf), acc, 0, 0, 0);
}
__device__ __forceinline__ f32x4 proj_step16(LAS char* L, int a, const u32x2& xf, f32x4 acc) {
  return __builtin_amdgcn_mfma_f32_16x16x16bf16_1k(__builtin_bit_cast(s16x4, tr_read(L, a)), __builtin_bit_cast(s16x4, xf), acc, 0, 0, 0);
}

__device__ __forceinline__ void qk_dots(const u32x4& k, const u32x4& q0, const u32x4& q1, float& d0, float& d1) {
  float a = 0.f, b = 0.f;
#pragma unroll
  for (int j = 0; j < 4; ++j) { a = dot2(k[j], q0[j], a); b = dot2(k[j], q1[j], b); }
  d0 = a; d1 = b;
}

template <typename F>
__device__ __forceinline__ void softmax_wave(float (&x)[32], LAS char* L, int wave, int lane, u32x4 (&pp)[4], F gap) {
#pragma unroll
  for (int v = 0; v < 16; ++v) x[v] = swap16_add(x[v], x[v + 16]);
  gap(0);
#define TR_STEP(H, CTRL, BIT) _Pragma("unroll") for (int v = 0; v < (H); ++v) { \
    const float t1 = x[v] + dpp<CTRL>(x[v]), t2 = x[v + (H)] + dpp<CTRL>(x[v + (H)]); x[v] = (lane & (BIT)) ? t2 : t1; }
  TR_STEP(8, ROR8, 8) TR_STEP(4, XOR7, 4) TR_STEP(2, XOR2, 2) TR_STEP(1, XOR1, 1)
#undef TR_STEP
  asm volatile("" : "+v"(x[0]));
  gap(1);
  const float t = rbf(x[0]) * (0.0625f * LOG2E);
  float m = fmaxf(t, dpp<XOR2>(t));
  m = fmaxf(m, dpp<ROR4>(m)); m = fmaxf(m, dpp<ROR8>(m)); m = swap32_max(swap16_max(m));
  const float p = __builtin_amdgcn_exp2f(t - (m == -INFINITY ? 0.f : m));
  const float po = dpp<XOR2>(p);
  const int pb = L_PB + wave * 128;
  if (!(lane & 2)) lds_st<unsigned>(L, pb + (((lane >> 5) * 2 + (lane & 1)) * 8 + ((lane & 31) >> 2)) * 4, pkbf(p, po));
  float l = p + po;
  l += dpp<ROR4>(l); l += dpp<ROR8>(l); l = swap16_add(l, l); l = swap32_add(l, l);
  if (lane < 2) lds_st<f32x2>(L, L_ML + wave * 16 + lane * 8, f32x2{m, l});
  asm volatile("s_waitcnt lgkmcnt(0)" ::: "memory");
#pragma unroll
  for (int j = 0; j < 4; ++j) pp[j] = lds_ld<u32x4>(L, pb + (lane >> 5) * 64 + j * 16);
}

__device__ __forceinline__ void pv_pair(const u32x4& v0, const u32x4& v1, unsigned p0, unsigned p1, float (&acc)[16]) {
#pragma unroll
  for (int k = 0; k < 4; ++k) {
    const unsigned lo = __builtin_amdgcn_perm(v1[k], v0[k], 0x05040100u), hi = __builtin_amdgcn_perm(v1[k], v0[k], 0x07060302u);
    acc[2 * k] = dot2(lo, p0, acc[2 * k]); acc[2 * k + 1] = dot2(hi, p0, acc[2 * k + 1]);
    acc[8 + 2 * k] = dot2(lo, p1, acc[8 + 2 * k]); acc[9 + 2 * k] = dot2(hi, p1, acc[9 + 2 * k]);
  }
}
__device__ __forceinline__ void store_part(float (&acc)[16], LAS char* L, int wave, int lane) {
  f32x4 a, b;
#pragma unroll
  for (int v = 0; v < 4; ++v) { a[v] = swap32_add(acc[v], acc[v + 8]); b[v] = swap32_add(acc[v + 4], acc[v + 12]); }
  lds_st<f32x4>(L, L_PART + wave * 2048 + lane * 32, a);
  lds_st<f32x4>(L, L_PART + wave * 2048 + lane * 32 + 16, b);
}
__device__ __forceinline__ void pair_rows(u32x4& r0, u32x4& r1) {
#pragma unroll
  for (int k = 0; k < 4; ++k) {
    const unsigned lo = __builtin_amdgcn_perm(r1[k], r0[k], 0x05040100u), hi = __builtin_amdgcn_perm(r1[k], r0[k], 0x07060302u);
    r0[k] = lo; r1[k] = hi;
  }
}
__device__ __forceinline__ void gemv_pair(const u32x4& lo, const u32x4& hi, unsigned a, float (&acc)[8]) {
#pragma unroll
  for (int k = 0; k < 4; ++k) { acc[2 * k] = dot2(lo[k], a, acc[2 * k]); acc[2 * k + 1] = dot2(hi[k], a, acc[2 * k + 1]); }
}
__device__ __forceinline__ unsigned unit_bad(const u32x4& u, unsigned tag) { return (u[1] ^ tag) | (u[3] ^ tag); }
__device__ __forceinline__ unsigned pack20(float v, unsigned tag12) { return ((asu(v) + 0x800u) & 0xfffff000u) | tag12; }
__device__ __forceinline__ float unpack20(unsigned w) { return asf(w & 0xfffff000u); }
__device__ __forceinline__ unsigned packed_bad(const u32x4& u, unsigned tag12) { return ((u[0] ^ tag12) | (u[1] ^ tag12) | (u[2] ^ tag12) | (u[3] ^ tag12)) & 0xfffu; }
template <int SLEEP> __device__ __forceinline__ void wait_set(LAS char* L, int set) {
  _Pragma("nounroll") for (unsigned it = 0; it < SPIN_CAP; ++it) {
    asm volatile("" ::: "memory");
    const u32x4 f = lds_ld<u32x4>(L, L_FLAG + set);
    if (__builtin_amdgcn_readfirstlane(f[0] & f[1] & f[2] & f[3])) break;
    if (SLEEP) __builtin_amdgcn_s_sleep(SLEEP);
  }
  asm volatile("" ::: "memory");
}
__device__ __forceinline__ void arrive(LAS char* L, int set, int pos, int lane) {
  asm volatile("s_waitcnt lgkmcnt(0)" ::: "memory");
  if (lane == 0) lds_st<unsigned>(L, L_FLAG + set + pos * 4, 1u);
}
__device__ __forceinline__ int kb_of(int u, int s) { return (u / GKB) * (8 * GKB) + s * GKB + (u % GKB); }
#endif

__global__ void __launch_bounds__(512, 2)
fused_decode(const unsigned short* __restrict__ x, const unsigned short* __restrict__ kc, const unsigned short* __restrict__ vc,
             const unsigned short* __restrict__ wq, const unsigned short* __restrict__ wo, const unsigned short* __restrict__ w1,
             const unsigned short* __restrict__ w2, const unsigned short* __restrict__ gw, unsigned short* __restrict__ out,
             char* ws) {
#if defined(__HIP_DEVICE_COMPILE__)
  __shared__ __attribute__((aligned(16))) char smem[L_END];
  LAS char* L = (LAS char*)smem;
  const int lane = threadIdx.x & 63, wave = __builtin_amdgcn_readfirstlane(threadIdx.x >> 6);
  const int bid = blockIdx.x, xl = bid & 7, r = bid >> 3, b = r >> 3;
  const int kvh = MAPX ? xl : (r & 7), s = MAPX ? (r & 7) : xl;
  const int grp = b * HKV + kvh;
  const bool red = s < 7;
  const int jf = red ? kvh * 7 + s : 0;
  const size_t hb = ((size_t)grp * SK + (SK - WIN)) * DH;
  const rsrc_t rk = mk_rsrc(kc + hb, WIN * DH * 2), rv = mk_rsrc(vc + hb, WIN * DH * 2);
  const rsrc_t rw = mk_rsrc(ws + WSOFF, WS_END);
  const int rot = ROTM == 0 ? 0 : ROTM == 1 ? ((bid * 37) & 31) * 4 : ROTM == 2 ? ((r + xl * 4) & 31) * 4 : ROTM == 3 ? ((r * 5 + xl) & 31) * 4 : ROTM == 4 ? (r & 7) * 4 : ((r + xl) & 7) * 4;
  const int w4 = wave & 3;
#define KV_SOFF(G, i) (kb_of(((G) * 64 + 4 * (i) + w4 + rot) & 127, s) * 1024)
  TS_DECL;
  TS(0);

#define DMA(rs, ldso, vo, so, aux) __builtin_amdgcn_raw_ptr_buffer_load_lds(rs, (LAS void*)(L + (ldso)), 16, vo, so, 0, aux)
  if (wave >= 4) {
    const int rg = lane >> 4, chunk = 16 * w4 + (lane & 15), colb = chunk < 56 ? chunk * 16 : OOB;
    const rsrc_t r2 = mk_rsrc(w2 + (size_t)(32 * jf) * DM, red ? 32 * DM * 2 : 0);
    const int v2 = rg * 8 * (DM * 2) + colb;
    u32x4 K[16], V[16], W2[8];
#pragma unroll
    for (int i = 0; i < 32; ++i) {
      if (i == BPRE) WG_BARRIER();
      if (i < 16) K[i] = ld16<AUX_NT>(rk, lane * 16, KV_SOFF(0, i));
      else V[i - 16] = ld16<AUX_NT>(rv, lane * 16, KV_SOFF(0, i - 16));
      FENCE();
      if ((i & 3) == 3) { W2[i >> 2] = ld16<WAUX>(r2, v2, (i >> 2) * DM * 2); FENCE(); }
    }
    arrive(L, F_ISS, w4, lane);
    TS(1);
    wait_set<4>(L, F_Q);
    TS(2);
    const u32x4 misc = lds_ld<u32x4>(L, L_MISC), hp = lds_ld<u32x4>(L, L_HS + rg * 16);
    const unsigned tag12 = misc[0] & 0xfffu;
    const u32x4 q0 = lds_ld<u32x4>(L, L_QS + (lane & 31) * 16), q1 = lds_ld<u32x4>(L, L_QS + 512 + (lane & 31) * 16);
    float sc[32];
#pragma unroll
    for (int i = 0; i < 16; ++i) qk_dots(K[i], q0, q1, sc[2 * i], sc[2 * i + 1]);
    u32x4 pp[4];
    softmax_wave(sc, L, wave, lane, pp, [](int) {});
    float acc[16];
#pragma unroll
    for (int v = 0; v < 16; ++v) acc[v] = 0.f;
#pragma unroll
    for (int u = 0; u < 8; ++u) pv_pair(V[2 * u], V[2 * u + 1], pp[u >> 2][u & 3], pp[2 + (u >> 2)][u & 3], acc);
    store_part(acc, L, wave, lane);
    arrive(L, F_STB, w4, lane);
    TS(3);
    float af[8], aa[8];
#pragma unroll
    for (int v = 0; v < 8; ++v) { af[v] = 0.f; aa[v] = 0.f; }
#pragma unroll
    for (int u = 0; u < 4; ++u) { pair_rows(W2[2 * u], W2[2 * u + 1]); gemv_pair(W2[2 * u], W2[2 * u + 1], hp[u], af); }
#pragma unroll
    for (int v = 0; v < 8; ++v) { af[v] *= asf(misc[2]); asm volatile("" : "+v"(af[v])); }
    const rsrc_t ro = mk_rsrc(wo + (size_t)(2 * kvh * DH + 32 * s) * DM, (DH + 32) * DM * 2);
    const int vo = ((rg >> 1) * DH + (rg & 1) * 16) * (DM * 2) + colb;
    const int unit2 = 2 * chunk + (lane >> 5);
    const int o2 = (!(lane & 16) && chunk < 56) ? WS_H2 + (((b * 28 + (unit2 >> 2)) * 64 + kvh * 8 + s) * 4 + (unit2 & 3)) * 16 : OOB;
    u32x4 WO[16];
#pragma unroll
    for (int i = 0; i < WOE; ++i) { FENCE(); WO[i] = ld16<WAUX>(ro, vo, i * DM * 2); FENCE(); }
    wait_set<2>(L, F_VI);
    TS(4);
#pragma unroll
    for (int i = WOE; i < 16; ++i) { FENCE(); WO[i] = ld16<WAUX>(ro, vo, i * DM * 2); FENCE(); }
    TS(5);
    const int j16 = lane & 15;
    const int os = WS_H1S + (grp * 16 + ((j16 >> 2) + 4 * (j16 & 1)) * 2 + (rg >> 1)) * 16;
    const int oa1 = WS_H1 + ((grp * 8 + s) * 128 + (j16 >> 2) * 16 + 4 * rg + (j16 & 3)) * 16;
#pragma unroll
    for (int u = 0; u < 8; ++u) { pair_rows(WO[2 * u], WO[2 * u + 1]); asm volatile("" : "+v"(WO[2 * u]), "+v"(WO[2 * u + 1])); }
    TS(6);
    wait_set<1>(L, F_PA);
    TS(7);
    if (D1) __builtin_amdgcn_s_sleep(D1);
    u32x4 a0, a1, st;
    _Pragma("nounroll") for (unsigned it = 0; it < SPIN_CAP; ++it) {
      st = ld_unit(rw, os, 0); a0 = ld_unit(rw, oa1, 0); a1 = ld_unit(rw, oa1, 1024);
      if (__all((unit_bad(st, misc[0]) | packed_bad(a0, tag12) | packed_bad(a1, tag12)) == 0)) break;
      if (PS1) __builtin_amdgcn_s_sleep(PS1);
    }
    TS(8);
    if (lane == 0) lds_st<unsigned>(L, L_FLAG + F_H2 + w4 * 4, 1u);
    const float Ms = asf(st[0]);
    float Mg = fmaxf(Ms, dpp<XOR1>(Ms)); Mg = fmaxf(Mg, dpp<ROR4>(Mg)); Mg = fmaxf(Mg, dpp<ROR8>(Mg));
    const float e = __builtin_amdgcn_exp2f(Ms - Mg);
    float Lg = e * asf(st[2]);
    Lg += dpp<XOR1>(Lg); Lg += dpp<ROR4>(Lg); Lg += dpp<ROR8>(Lg);
    const float wgt = e * __builtin_amdgcn_rcpf(Lg);
    const float wa = dpp<0x00>(wgt), wb = dpp<0x55>(wgt);
    float v[4];
#pragma unroll
    for (int k = 0; k < 4; ++k) { v[k] = wa * unpack20(a0[k]) + wb * unpack20(a1[k]); v[k] += dpp<ROR4>(v[k]); v[k] += dpp<ROR8>(v[k]); }
    const float p0 = asf(pkbf(v[0], v[1])), p1 = asf(pkbf(v[2], v[3]));
    gemv_pair(WO[0], WO[1], asu(dpp<0x00>(p0)), aa); gemv_pair(WO[2], WO[3], asu(dpp<0x00>(p1)), aa);
    gemv_pair(WO[4], WO[5], asu(dpp<0x55>(p0)), aa); gemv_pair(WO[6], WO[7], asu(dpp<0x55>(p1)), aa);
    gemv_pair(WO[8], WO[9], asu(dpp<0xAA>(p0)), aa); gemv_pair(WO[10], WO[11], asu(dpp<0xAA>(p1)), aa);
    gemv_pair(WO[12], WO[13], asu(dpp<0xFF>(p0)), aa); gemv_pair(WO[14], WO[15], asu(dpp<0xFF>(p1)), aa);
    float z[4];
#pragma unroll
    for (int v = 0; v < 4; ++v) {
      z[v] = swap32_add(fmaf(aa[v], asf(misc[1]), af[v]), fmaf(aa[v + 4], asf(misc[1]), af[v + 4]));
      z[v] = swap16_add(z[v], z[v]);
    }
    st16<AUX_SC1>(rw, o2, u32x4{pack20(z[0], tag12), pack20(z[1], tag12), pack20(z[2], tag12), pack20(z[3], tag12)});
    TS(9);
    TS_FLUSH();
    return;
  }

  unsigned tag;
  {
    const int cg = bid >> 2, kq = bid & 3;
    const bool hv = bid < 196;
    const int ch = hv ? bid / 7 : 0, kh = hv ? bid % 7 : 0;
    const int n = lane & 15, g = lane >> 4;
    const rsrc_t rq = mk_rsrc(wq + (size_t)kq * 112 * NQ + 64 * cg, (111 * NQ + 64) * 2);
    const rsrc_t r1 = mk_rsrc(w1 + (size_t)kh * 64 * DFF + 64 * ch, hv ? (63 * DFF + 64) * 2 : 0);
#pragma unroll
    for (int j = 0; j < 4; ++j)
      if (4 * j + wave < 14) DMA(rq, L_WQ + (4 * j + wave) * 1024, (lane >> 3) * (NQ * 2) + (lane & 7) * 16, (4 * j + wave) * 8 * NQ * 2, PAUX);
#pragma unroll
    for (int j = 0; j < 2; ++j) DMA(r1, L_W1 + (4 * j + wave) * 1024, (lane >> 3) * (DFF * 2) + (lane & 7) * 16, (4 * j + wave) * 8 * DFF * 2, PAUX);
    DMA(mk_rsrc(x, NB * DM * 2), L_X + wave * 1024, lane * 16, wave * 1024, 0);
    const unsigned tagn = __builtin_amdgcn_raw_buffer_load_b32(rw, WS_EPOCH + (lane & 3) * 4, 0, AUX_SC1) + 1u;
    u32x4 gv;
    if (wave == 3) gv = ld16<0>(mk_rsrc(gw, DM * 2), lane * 16, 0);
    if (lane < 8) lds_st<unsigned>(L, L_FLAG + lane * 16 + wave * 4, 0u);
    FENCE();
    WG_BARRIER();
    WAIT_VM0();
    TS(1);
    arrive(L, F_PRO, wave, lane);
    wait_set<0>(L, F_PRO);
    const unsigned t12n = tagn & 0xfffu;
    const int col = wave * 32 + (lane & 3) * 8, xa = L_X + (n & 3) * (DM * 2);
    f32x4 aq = {0.f, 0.f, 0.f, 0.f}, ah = {0.f, 0.f, 0.f, 0.f};
#pragma unroll
    for (int t = 0; t < 3; ++t) aq = proj_step32(L, L_WQ + (32 * t + 8 * g + (n >> 2)) * 128 + col, lds_ld<u32x4>(L, xa + kq * 224 + t * 64 + g * 16), aq);
    aq += proj_step16(L, L_WQ + (96 + 4 * g + (n >> 2)) * 128 + col, lds_ld<u32x2>(L, xa + kq * 224 + 192 + g * 8), f32x4{0.f, 0.f, 0.f, 0.f});
#pragma unroll
    for (int t = 0; t < 2; ++t) ah = proj_step32(L, L_W1 + (32 * t + 8 * g + (n >> 2)) * 128 + col, lds_ld<u32x4>(L, xa + kh * 128 + t * 64 + g * 16), ah);
    st16<H0AUX>(rw, n < NB ? WS_HQ + ((n * 4 + kq) * 1024 + cg * 16 + wave * 4 + g) * 16 : OOB,
                u32x4{pack20(aq[0], t12n), pack20(aq[1], t12n), pack20(aq[2], t12n), pack20(aq[3], t12n)});
    st16<H0AUX>(rw, n < NB && hv ? WS_HH + ((n * 7 + kh) * 448 + ch * 16 + wave * 4 + g) * 16 : OOB,
                u32x4{pack20(ah[0], t12n), pack20(ah[1], t12n), pack20(ah[2], t12n), pack20(ah[3], t12n)});
    TS(2);
    tag = __builtin_amdgcn_readlane(tagn, b);
    const unsigned tag12 = tag & 0xfffu;
    if (wave == 3) {
      const u32x4 xv = lds_ld<u32x4>(L, L_X + b * (DM * 2) + (lane < 56 ? lane : 0) * 16);
      float t = 0.f;
#pragma unroll
      for (int k = 0; k < 4; ++k) {
        t += rbf(asf(xv[k] << 16) * asf(gv[k] << 16));
        t += rbf(asf(xv[k] & 0xffff0000u) * asf(gv[k] & 0xffff0000u));
      }
      t = lane < 56 ? t : 0.f;
      t += dpp<XOR1>(t); t += dpp<XOR2>(t); t += dpp<ROR4>(t); t += dpp<ROR8>(t);
      t = swap16_add(t, t); t = swap32_add(t, t);
      const float gt = rbf(1.f / (1.f + __expf(-rbf(t))));
      if (lane == 0) lds_st<u32x4>(L, L_MISC, u32x4{tag, asu(gt), asu(rbf(1.f - gt)), 0u});
    }
    u32x4 a0, a1, ha;
    const int oq = WS_HQ + ((b * 4 + (lane >> 5)) * 1024 + kvh * 128 + wave * 32 + (lane & 31)) * 16;
    const int oh = wave == 0 && lane < 56 ? WS_HH + ((b * 7 + (lane >> 3)) * 448 + jf * 8 + (lane & 7)) * 16 : OOB;
    if (D0) __builtin_amdgcn_s_sleep(D0);
    _Pragma("nounroll") for (unsigned it = 0; it < SPIN_CAP; ++it) {
      asm volatile("" ::: "memory");
      a0 = ld_unit(rw, oq, 0); a1 = ld_unit(rw, oq, 2 * 1024 * 16); ha = ld_unit(rw, oh, 0);
      if (__all((packed_bad(a0, tag12) | packed_bad(a1, tag12) | (oh == OOB ? 0u : packed_bad(ha, tag12))) == 0)) break;
      __builtin_amdgcn_s_sleep(8);
    }
    {
      const float y0 = swap32_add(unpack20(a0[0]) + unpack20(a1[0]), unpack20(a0[2]) + unpack20(a1[2]));
      const float y1 = swap32_add(unpack20(a0[1]) + unpack20(a1[1]), unpack20(a0[3]) + unpack20(a1[3]));
      lds_st<unsigned>(L, L_QS + (wave * 32 + (lane & 31)) * 8 + (lane >> 5) * 4, pkbf(y0, y1));
    }
    if (wave == 0) {
      float y0 = swap32_add(unpack20(ha[0]), unpack20(ha[2])), y1 = swap32_add(unpack20(ha[1]), unpack20(ha[3]));
      y0 = swap16_add(y0, y0); y1 = swap16_add(y1, y1);
      y0 += dpp<ROR8>(y0); y1 += dpp<ROR8>(y1);
      if (!(lane & 24)) lds_st<unsigned>(L, L_HS + (lane & 7) * 8 + (lane >> 5) * 4, pkbf(y0 < 0.f ? 0.f : y0, y1 < 0.f ? 0.f : y1));
    }
    arrive(L, F_Q, wave, lane);
    TS(3);
  }
  wait_set<4>(L, F_ISS);
  TS(5);
  {
    u32x4 K[16], V[16];
#pragma unroll
    for (int i = 0; i < 16; ++i) { K[i] = ld16<AUX_NT>(rk, lane * 16, KV_SOFF(1, i)); FENCE(); }
#define LDV(i) do { if ((i) < 16) { FENCE(); V[(i) & 15] = ld16<AUX_NT>(rv, lane * 16, KV_SOFF(1, (i) & 15)); FENCE(); } } while (0)
#pragma unroll
    for (int i = 0; i < LAG; ++i) LDV(i);
    wait_set<1>(L, F_Q);
    const u32x4 q0 = lds_ld<u32x4>(L, L_QS + (lane & 31) * 16), q1 = lds_ld<u32x4>(L, L_QS + 512 + (lane & 31) * 16);
    float sc[32];
#pragma unroll
    for (int i = 0; i < 8; ++i) {
      LDV(LAG + i);
      qk_dots(K[2 * i], q0, q1, sc[4 * i], sc[4 * i + 1]);
      qk_dots(K[2 * i + 1], q0, q1, sc[4 * i + 2], sc[4 * i + 3]);
      FENCE();
    }
    TS(6);
    u32x4 pp[4];
    static_assert(LAG >= 6, "every V load is issued by the end of the softmax");
    softmax_wave(sc, L, wave, lane, pp, [&](int k) {
      LDV(LAG + 8 + k);
      if (k == 1 && lane == 0) lds_st<unsigned>(L, L_FLAG + F_VI + wave * 4, 1u);
    });
    if (lane == 0) lds_st<unsigned>(L, L_FLAG + F_STA + wave * 4, 1u);
    TS(7);
    float acc[16];
#pragma unroll
    for (int v = 0; v < 16; ++v) acc[v] = 0.f;
#define PV(u) do { pv_pair(V[2 * (u)], V[2 * (u) + 1], pp[(u) >> 2][(u) & 3], pp[2 + ((u) >> 2)][(u) & 3], acc); \
      asm volatile("" : "+v"(acc[0]), "+v"(acc[1]), "+v"(acc[2]), "+v"(acc[3]), "+v"(acc[4]), "+v"(acc[5]), "+v"(acc[6]), "+v"(acc[7]), \
                        "+v"(acc[8]), "+v"(acc[9]), "+v"(acc[10]), "+v"(acc[11]), "+v"(acc[12]), "+v"(acc[13]), "+v"(acc[14]), "+v"(acc[15])); } while (0)
#pragma unroll
    for (int u = 0; u < 5; ++u) PV(u);
    const bool mrg = wave == 1 || wave == 2;
    const int hd = wave & 1, pf = L_PART + hd * 1024 + lane * 16;
    float wt[8], M = -INFINITY, Ls = 0.f;
    f32x4 sum = {0.f, 0.f, 0.f, 0.f};
    if (mrg) {
      wait_set<0>(L, F_STA);
      wait_set<0>(L, F_STB);
      f32x2 ml[8];
#pragma unroll
      for (int w = 0; w < 8; ++w) { ml[w] = lds_ld<f32x2>(L, L_ML + w * 16 + hd * 8); M = fmaxf(M, ml[w][0]); }
#pragma unroll
      for (int w = 0; w < 8; ++w) { wt[w] = __builtin_amdgcn_exp2f(ml[w][0] - (M == -INFINITY ? 0.f : M)); Ls = fmaf(wt[w], ml[w][1], Ls); }
#pragma unroll
      for (int w = 4; w < 8; ++w) sum += wt[w] * lds_ld<f32x4>(L, pf + w * 2048);
      asm volatile("" : "+v"(sum), "+v"(wt[0]), "+v"(wt[1]), "+v"(wt[2]), "+v"(wt[3]), "+v"(Ls));
    }
    TS(8);
#pragma unroll
    for (int u = 5; u < 8; ++u) PV(u);
    store_part(acc, L, wave, lane);
    arrive(L, F_PA, wave, lane);
    TS(9);
    if (mrg) {
      st_unit(rw, lane == 0 ? WS_H1S + (grp * 16 + s * 2 + hd) * 16 : OOB, asu(M), asu(Ls), tag);
      wait_set<0>(L, F_PA);
      const unsigned tag12 = tag & 0xfffu;
#pragma unroll
      for (int w = 0; w < 4; ++w) sum += wt[w] * lds_ld<f32x4>(L, pf + w * 2048);
      st16<AUX_SC1>(rw, WS_H1 + (((grp * 8 + (lane >> 3)) * 8 + s) * 16 + hd * 8 + (lane & 7)) * 16,
                    u32x4{pack20(sum[0], tag12), pack20(sum[1], tag12), pack20(sum[2], tag12), pack20(sum[3], tag12)});
    }
    TS(10);
  }
  const int jr = s * 8 + kvh;
  if (wave == 0 && jr < 28) {
    const unsigned tag12 = tag & 0xfffu;
    wait_set<1>(L, F_H2);
    TS(12);
    u32x4 a[4];
    const int oa = WS_H2 + (b * 28 + jr) * 4096 + lane * 16;
    if (D2) __builtin_amdgcn_s_sleep(D2);
    _Pragma("nounroll") for (unsigned it = 0; it < SPIN_CAP; ++it) {
      asm volatile("" ::: "memory");
#pragma unroll
      for (int i = 0; i < 4; ++i) a[i] = ld_unit(rw, oa, i * 1024);
      if (__all((packed_bad(a[0], tag12) | packed_bad(a[1], tag12) | packed_bad(a[2], tag12) | packed_bad(a[3], tag12)) == 0)) break;
      if (PS2) __builtin_amdgcn_s_sleep(PS2);
    }
    TS(13);
    float v[4];
#pragma unroll
    for (int k = 0; k < 4; ++k) v[k] = (unpack20(a[0][k]) + unpack20(a[1][k])) + (unpack20(a[2][k]) + unpack20(a[3][k]));
    float y = swap32_add(swap16_add(v[0], v[1]), swap16_add(v[2], v[3]));
    y += dpp<ROR4>(y); y += dpp<ROR8>(y);
    if (!(lane & 12)) out[b * DM + 16 * jr + 4 * (lane & 3) + (lane >> 4)] = (unsigned short)pkbf(y, y);
    if (jr == 0 && lane == 0) __builtin_amdgcn_raw_buffer_store_b32(tag, rw, WS_EPOCH + b * 4, 0, AUX_SC1);
    TS(14);
  }
  TS_FLUSH();
#endif
}

extern "C" void kernel_launch(void* const* d_in, const int* in_sizes, int n_in, void* d_out, int out_size, void* d_ws,
                              size_t ws_size, hipStream_t stream) {
  typedef const unsigned short* P;
  fused_decode<<<256, 512, 0, stream>>>((P)d_in[0], (P)d_in[1], (P)d_in[2], (P)d_in[3], (P)d_in[4], (P)d_in[6], (P)d_in[7],
                                        (P)d_in[8], (unsigned short*)d_out, (char*)d_ws);
}
```

```cpp
#include <hip/hip_runtime.h>
#include <stdint.h>
#include <type_traits>

typedef unsigned u32x4 __attribute__((ext_vector_type(4)));
typedef unsigned u32x2 __attribute__((ext_vector_type(2)));
typedef float f32x4 __attribute__((ext_vector_type(4)));
typedef float f32x2 __attribute__((ext_vector_type(2)));
typedef short s16x4 __attribute__((ext_vector_type(4)));
typedef __bf16 bf16x2 __attribute__((ext_vector_type(2)));
typedef __bf16 bf16x8 __attribute__((ext_vector_type(8)));
#define LAS __attribute__((address_space(3)))
typedef __amdgpu_buffer_rsrc_t rsrc_t;

constexpr int NB = 4, DM = 448, HKV = 8, SK = 8192, DH = 256, WIN = 2048, DFF = 1792, NQ = 4096;
constexpr int KS = 2;

constexpr int SP = 140;
constexpr int SN = 90;
constexpr int TMID = 619;
constexpr int TEND = 1072;
constexpr int TQ = 410;
constexpr int TQL = 600;
constexpr int RCOMP = 12;
constexpr int J0 = 17;
constexpr int PJ = 19;
constexpr int DLAG = 8;
constexpr int EI = 2;
constexpr int BTI = 8;
constexpr int D1 = 12;
constexpr int X1 = 142;
constexpr int D2 = 15;
constexpr int DS = 20;

constexpr int WS_EPOCH = 0;
constexpr int WS_T0 = 1024;
constexpr int WS_H0 = 2048;
constexpr int WS_H1S = WS_H0 + NB * KS * 1472 * 16;
constexpr int WS_H1 = WS_H1S + NB * HKV * 16 * 16;
constexpr int WS_H2 = WS_H1 + NB * HKV * 64 * 16 * 16;
constexpr int WS_END = WS_H2 + NB * 28 * 64 * 64;

constexpr int L_WP = 0;
constexpr int L_PART = L_WP + 57344 / KS;
constexpr int L_X = L_PART + 8192;
constexpr int L_QS = L_X + 4096;
constexpr int L_HS = L_QS + 1024;
constexpr int L_PB = L_HS + 64;
constexpr int L_ML = L_PB + 1024;
constexpr int L_FLAG = L_ML + 128;
constexpr int F_PRO = 0, F_M = 16, F_P = 32, F_ISS = 48, F_Q = 64, F_VI = 80, F_PA = 96, F_H2 = 112, F_W = 128, F_LEAD = 144;
constexpr int L_MISC = L_FLAG + 160;
constexpr int L_END = L_MISC + 16;

constexpr int AUX_NT = 2, AUX_SC1 = 16, OOB = 0x40000000;
constexpr unsigned SPIN_CAP = 1u << 19;
constexpr float LOG2E = 1.4426950408889634f;

#if defined(__HIP_DEVICE_COMPILE__)
__device__ __forceinline__ rsrc_t mk_rsrc(const void* p, unsigned bytes) {
  return __builtin_amdgcn_make_buffer_rsrc((void*)p, 0, bytes, 0x00020000);
}
template <int AUX> __device__ __forceinline__ u32x4 ld16(rsrc_t r, int voff, int soff) {
  return __builtin_amdgcn_raw_buffer_load_b128(r, voff, soff, AUX);
}
__device__ __forceinline__ u32x4 ld_unit(rsrc_t r, int voff, int soff) {
  asm volatile("" : "+v"(voff));
  return __builtin_amdgcn_raw_buffer_load_b128(r, voff, soff, AUX_SC1);
}
__device__ __forceinline__ float asf(unsigned u) { return __builtin_bit_cast(float, u); }
__device__ __forceinline__ unsigned asu(float f) { return __builtin_bit_cast(unsigned, f); }
__device__ __forceinline__ void st_wide(rsrc_t r, int voff, unsigned a, unsigned b, unsigned tag) {
  __builtin_amdgcn_raw_buffer_store_b128(u32x4{a, tag, b, tag}, r, voff, 0, AUX_SC1);
}
__device__ __forceinline__ u32x4 check_bits(unsigned tag) {
  const unsigned t = tag & 0xfffu;
  return u32x4{t, t ^ ((tag >> 12) & 0xfffu), t, t ^ (tag >> 20)};
}
__device__ __forceinline__ void st_packed(rsrc_t r, int voff, f32x4 v, const u32x4& chk) {
  u32x4 w;
#pragma unroll
  for (int k = 0; k < 4; ++k) asm("v_and_or_b32 %0, %1, %2, %3" : "=v"(w[k]) : "v"(asu(v[k]) + 0x800u), "s"(0xfffff000u), "v"(chk[k]));
  __builtin_amdgcn_raw_buffer_store_b128(w, r, voff, 0, AUX_SC1);
}
__device__ __forceinline__ unsigned wide_bad(const u32x4& u, unsigned tag) { return (u[1] ^ tag) | (u[3] ^ tag); }
__device__ __forceinline__ unsigned strip(u32x4& u, const u32x4& chk) {
  u ^= chk;
  return (u[0] | u[1] | u[2] | u[3]) & 0xfffu;
}
__device__ __forceinline__ unsigned pkbf(float lo, float hi) {
  return __builtin_bit_cast(unsigned, __builtin_convertvector((f32x2{lo, hi}), bf16x2));
}
__device__ __forceinline__ float rbf(float f) { return asf(pkbf(f, f) << 16); }
__device__ __forceinline__ float dot2(unsigned a, unsigned b, float acc) {
  return __builtin_amdgcn_fdot2_f32_bf16(__builtin_bit_cast(bf16x2, a), __builtin_bit_cast(bf16x2, b), acc, false);
}
template <int CTRL> __device__ __forceinline__ int dppi(int x) { return __builtin_amdgcn_mov_dpp(x, CTRL, 0xf, 0xf, true); }
template <int CTRL> __device__ __forceinline__ float dpp(float x) { return asf(dppi<CTRL>(asu(x))); }
constexpr int XOR1 = 0xB1, XOR2 = 0x4E, XOR7 = 0x141, ROR8 = 0x128, ROR4 = 0x124;
__device__ __forceinline__ float swap16_add(float a, float b) {
  auto s = __builtin_amdgcn_permlane16_swap(asu(a), asu(b), false, false);
  return asf(s[0]) + asf(s[1]);
}
__device__ __forceinline__ float swap32_add(float a, float b) {
  auto s = __builtin_amdgcn_permlane32_swap(asu(a), asu(b), false, false);
  return asf(s[0]) + asf(s[1]);
}
__device__ __forceinline__ float rows_max(float a) {
  auto s = __builtin_amdgcn_permlane16_swap(asu(a), asu(a), false, false);
  a = fmaxf(asf(s[0]), asf(s[1]));
  auto t = __builtin_amdgcn_permlane32_swap(asu(a), asu(a), false, false);
  return fmaxf(asf(t[0]), asf(t[1]));
}
template <typename T> __device__ __forceinline__ T lds_ld(LAS char* L, int off) { return *(LAS T*)(L + off); }
template <typename T> __device__ __forceinline__ void lds_st(LAS char* L, int off, T v) { *(LAS T*)(L + off) = v; }
template <int I, int N, typename F> __device__ __forceinline__ void static_for(F f) {
  if constexpr (I < N) { f(std::integral_constant<int, I>{}); static_for<I + 1, N>(f); }
}
#define FENCE() __builtin_amdgcn_sched_barrier(0)
#define WAIT_VM0() asm volatile("s_waitcnt vmcnt(0)" ::: "memory")
#define DMA(rs, ldso, vo, so, aux) __builtin_amdgcn_raw_ptr_buffer_load_lds(rs, (LAS void*)(L + (ldso)), 16, vo, so, 0, aux)

template <int SLEEP> __device__ __forceinline__ void wait_set(LAS char* L, int set) {
#pragma nounroll
  for (unsigned it = 0; it < SPIN_CAP; ++it) {
    asm volatile("" ::: "memory");
    const u32x4 f = lds_ld<u32x4>(L, L_FLAG + set);
    if (__builtin_amdgcn_readfirstlane(f[0] & f[1] & f[2] & f[3])) break;
    if (SLEEP) __builtin_amdgcn_s_sleep(SLEEP);
  }
  asm volatile("" ::: "memory");
}
__device__ __forceinline__ void signal(LAS char* L, int set, int pos, int lane) {
  if (lane == 0) lds_st<unsigned>(L, L_FLAG + set + pos * 4, 1u);
}
__device__ __forceinline__ void arrive(LAS char* L, int set, int pos, int lane) {
  asm volatile("s_waitcnt lgkmcnt(0)" ::: "memory");
  signal(L, set, pos, lane);
}

__device__ __forceinline__ unsigned clock100() {
  unsigned long long t;
  asm volatile("s_memrealtime %0\n\ts_waitcnt lgkmcnt(0)" : "=s"(t) :: "memory");
  return (unsigned)t;
}
template <int TICKS> __device__ __forceinline__ void wait_until(unsigned base) {
  while (clock100() - base < (unsigned)TICKS) __builtin_amdgcn_s_sleep(1);
}
__device__ __forceinline__ unsigned pace(unsigned base, unsigned& next, unsigned dt) {
  unsigned d;
  asm volatile("s_add_u32 %1, %1, %3\n\ts_branch 2f\n1:\n\ts_sleep 1\n2:\n\ts_memrealtime vcc\n\ts_waitcnt lgkmcnt(0)\n\ts_sub_u32 %0, vcc_lo, %2\n\t"
               "s_lshl_b32 %0, %0, 4\n\ts_cmp_lt_u32 %0, %1\n\ts_cbranch_scc1 1b"
               : "=&s"(d), "+s"(next) : "s"(base), "s"(dt) : "vcc", "scc", "memory");
  return d >> 4;
}
constexpr unsigned step_k(int start) { return (unsigned)((TMID - start) * 16 / 32); }
constexpr unsigned STEP_V = (unsigned)((TEND - TMID) * 16 / 31);
constexpr int pairs_done(int i) { return i < J0 ? 0 : (i - J0 + 1 < (i - DLAG + 1) / 2 ? i - J0 + 1 : (i - DLAG + 1) / 2); }

#define NEW_PAGE(n) asm volatile("s_branch .Lpage_go" #n "\n\t.p2align 12\n.Lpage" #n ":\n\ts_setpc_b64 vcc\n.Lpage_go" #n ":" ::: "memory")
#define TOUCH_PAGE(n) asm volatile("s_getpc_b64 vcc\n.Lpage_h" #n ":\n\ts_add_u32 vcc_lo, vcc_lo, .Lpage_r" #n "-.Lpage_h" #n \
                                   "\n\ts_addc_u32 vcc_hi, vcc_hi, 0\n\ts_branch .Lpage" #n "\n.Lpage_r" #n ":" ::: "vcc", "scc", "memory")

__device__ __forceinline__ f32x4 proj_step(LAS char* L, int a, const u32x4& xf, f32x4 acc) {
  const u32x2 lo = __builtin_bit_cast(u32x2, __builtin_amdgcn_ds_read_tr16_b64_v4i16((LAS s16x4*)(L + a)));
  const u32x2 hi = __builtin_bit_cast(u32x2, __builtin_amdgcn_ds_read_tr16_b64_v4i16((LAS s16x4*)(L + a + 4 * 128)));
  const u32x4 w = {lo[0], lo[1], hi[0], hi[1]};
  return __builtin_amdgcn_mfma_f32_16x16x32_bf16(__builtin_bit_cast(bf16x8, w), __builtin_bit_cast(bf16x8, xf), acc, 0, 0, 0);
}

__device__ __forceinline__ float scores16(const u32x4* K, const u32x4& q0, const u32x4& q1, int lane) {
  float x[32];
#pragma unroll
  for (int i = 0; i < 16; ++i) {
    float a = 0.f, b = 0.f;
#pragma unroll
    for (int j = 0; j < 4; ++j) { a = dot2(K[i][j], q0[j], a); b = dot2(K[i][j], q1[j], b); }
    x[2 * i] = a; x[2 * i + 1] = b;
  }
#pragma unroll
  for (int v = 0; v < 16; ++v) x[v] = swap16_add(x[v], x[v + 16]);
#define TR_STEP(H, CTRL) _Pragma("unroll") for (int v = 0; v < (H); ++v) { \
    const float t1 = x[v] + dpp<CTRL>(x[v]), t2 = x[v + (H)] + dpp<CTRL>(x[v + (H)]); x[v] = (lane & (H)) ? t2 : t1; }
  TR_STEP(8, ROR8) TR_STEP(4, XOR7) TR_STEP(2, XOR2) TR_STEP(1, XOR1)
#undef TR_STEP
  return rbf(x[0]) * (0.0625f * LOG2E);
}

__device__ __forceinline__ void pair_rows(u32x4& r0, u32x4& r1) {
#pragma unroll
  for (int k = 0; k < 4; ++k) {
    const unsigned lo = __builtin_amdgcn_perm(r1[k], r0[k], 0x05040100u), hi = __builtin_amdgcn_perm(r1[k], r0[k], 0x07060302u);
    r0[k] = lo; r1[k] = hi;
  }
}
__device__ __forceinline__ void gemv_pair(const u32x4& lo, const u32x4& hi, unsigned a, float* acc) {
#pragma unroll
  for (int k = 0; k < 4; ++k) { acc[2 * k] = dot2(lo[k], a, acc[2 * k]); acc[2 * k + 1] = dot2(hi[k], a, acc[2 * k + 1]); }
}
#endif

__global__ void __launch_bounds__(512, 2)
fused_decode(const unsigned short* __restrict__ x, const unsigned short* __restrict__ kc, const unsigned short* __restrict__ vc,
             const unsigned short* __restrict__ wq, const unsigned short* __restrict__ wo, const unsigned short* __restrict__ w1,
             const unsigned short* __restrict__ w2, const unsigned short* __restrict__ gw, unsigned short* __restrict__ out, char* ws) {
#if defined(__HIP_DEVICE_COMPILE__)
  __shared__ __attribute__((aligned(16))) char smem[L_END];
  LAS char* L = (LAS char*)smem;
  const int lane = threadIdx.x & 63, wave = __builtin_amdgcn_readfirstlane(threadIdx.x >> 6), w4 = wave & 3;
  const int bid = blockIdx.x, r = bid >> 3, b = r >> 3, s = bid & 7, kvh = r & 7;
  const unsigned base = (unsigned)__builtin_amdgcn_s_memrealtime() - (unsigned)(r * RCOMP) / 16u;
  if (threadIdx.x < 37) lds_st<unsigned>(L, L_FLAG + threadIdx.x * 4, threadIdx.x > 32 && threadIdx.x < 36 ? 1u : 0u);
  asm volatile("" :: "s"(x), "s"(kc), "s"(vc), "s"(wq), "s"(wo), "s"(w1), "s"(w2), "s"(gw), "s"(out), "s"(ws), "s"(base));
  const rsrc_t rw = mk_rsrc(ws, WS_END);
  const unsigned epoch = __builtin_amdgcn_raw_buffer_load_b32(rw, WS_EPOCH + (lane & 3) * 4, 0, AUX_SC1);
  FENCE();
  const bool prod = bid < 92 * KS, isq = bid < 64 * KS;
  const int cg = bid / KS, ks = bid % KS;
  if (wave < 4 && prod) {
    const int rowb = isq ? NQ * 2 : DFF * 2;
    const char* tp = (isq ? (const char*)wq + cg * 128 : (const char*)w1 + (cg - 64) * 128) + (size_t)ks * (DM / KS) * rowb;
    const rsrc_t rp = mk_rsrc(tp, (DM / KS - 1) * rowb + 128);
    const int vp = (lane >> 3) * rowb + (lane & 7) * 16;
#pragma nounroll
    for (int j = 0; j < 14 / KS; ++j) DMA(rp, L_WP + (4 * j + wave) * 1024, vp, (4 * j + wave) * 8 * rowb, AUX_NT);
    DMA(mk_rsrc(x, NB * DM * 2), L_X + wave * 1024, lane * 16, wave * 1024, 0);
  }
  FENCE();
  asm volatile("s_waitcnt lgkmcnt(0)\n\ts_barrier" ::: "memory");
  const size_t hb = ((size_t)r * SK + (SK - WIN)) * DH;
  int kvo[4];
#pragma unroll
  for (int i = 0; i < 4; ++i) kvo[i] = (((s >> 1) << 5 | ((((s & 1) << 1) + (w4 << 3) + (i & 1) + ((i & 2) << 1) + 7 * r) & 31)) << 10) + lane * 16;
  const bool ffn = s < 7;
  const int jf = ffn ? kvh * 7 + s : 0;
  int role = wave;
  asm volatile("" : "+s"(role));

  if (role < 4) {
    unsigned tag, tag0;
    {
      const int n = lane & 15, g = lane >> 4;
      WAIT_VM0();
      const unsigned tagn = epoch + 1u;
      if (prod) {
        signal(L, F_PRO, wave, lane);
        wait_set<0>(L, F_PRO);
        const int wa = L_WP + (8 * g + (n >> 2)) * 128 + wave * 32 + (lane & 3) * 8, xa = L_X + (n & 3) * (DM * 2) + ks * (DM * 2 / KS) + g * 16;
        f32x4 a = {0.f, 0.f, 0.f, 0.f};
#pragma unroll
        for (int t = 0; t < 14 / KS; ++t) a = proj_step(L, wa + t * 32 * 128, lds_ld<u32x4>(L, xa + t * 64), a);
        st_packed(rw, n < NB ? WS_H0 + ((n * KS + ks) * 1472 + cg * 16 + wave * 4 + g) * 16 : OOB, a, check_bits(tagn));
      }
      if (wave == 1 && bid < 64) st_wide(rw, lane == 0 ? WS_T0 + bid * 16 : OOB, base, base, tagn);
      tag0 = __builtin_amdgcn_readlane(tagn, 0);
      tag = __builtin_amdgcn_readlane(tagn, b);
    }
    const u32x4 chk = check_bits(tag);
    const int o0 = WS_H0 + (b * KS * 1472 + (wave < 2 ? kvh * 128 + wave * 64 + lane : 1024 + jf * 8 + (lane & 7))) * 16;
    auto gather_q = [&](unsigned limit) {
      u32x4 a[KS];
      bool ok = false;
#pragma nounroll
      for (unsigned it = 0; it < SPIN_CAP; ++it) {
        unsigned bad = 0;
#pragma unroll
        for (int k = 0; k < KS; ++k) a[k] = ld_unit(rw, o0, k * 1472 * 16);
#pragma unroll
        for (int k = 0; k < KS; ++k) bad |= strip(a[k], chk);
        ok = __all(bad == 0);
        if (ok || clock100() - base > limit) break;
        __builtin_amdgcn_s_sleep(4);
      }
      if (ok || limit == ~0u) {
        float y[4];
#pragma unroll
        for (int v = 0; v < 4; ++v) {
          y[v] = asf(a[0][v]);
#pragma unroll
          for (int k = 1; k < KS; ++k) y[v] += asf(a[k][v]);
          if (wave == 2) y[v] = y[v] < 0.f ? 0.f : y[v];
        }
        if (wave < 2 || lane < 8) lds_st<u32x2>(L, L_QS + wave * 512 + lane * 8, u32x2{pkbf(y[0], y[1]), pkbf(y[2], y[3])});
        arrive(L, F_Q, wave, lane);
      }
      return ok;
    };
    const int jr = s * 8 + kvh;
    unsigned short* po = out + b * DM + 16 * jr + 4 * (lane & 3) + (lane >> 4);
    asm volatile("" : "+v"(po));
    if (wave == 0 && jr < 28 && !(lane & 12)) *(__attribute__((address_space(1))) unsigned short*)po = 0;
    bool have_q = true;
    TOUCH_PAGE(2);
    TOUCH_PAGE(3);
    if (wave == 3) {
      const u32x4 gv = ld16<0>(mk_rsrc(gw, DM * 2), lane * 16, 0), xv = ld16<0>(mk_rsrc(x + b * DM, DM * 2), lane * 16, 0);
      float t = 0.f;
#pragma unroll
      for (int k = 0; k < 4; ++k) {
        t += rbf(asf(xv[k] << 16) * asf(gv[k] << 16));
        t += rbf(asf(xv[k] & 0xffff0000u) * asf(gv[k] & 0xffff0000u));
      }
      t += dpp<XOR1>(t); t += dpp<XOR2>(t); t += dpp<ROR4>(t); t += dpp<ROR8>(t);
      t = swap16_add(t, t); t = swap32_add(t, t);
      const float gt = rbf(1.f / (1.f + __expf(-rbf(t))));
      if (lane == 0) lds_st<u32x4>(L, L_MISC, u32x4{tag, asu(gt), asu(rbf(1.f - gt)), 0u});
      arrive(L, F_Q, wave, lane);
      wait_until<TQ + 40>(base);
      const u32x4 t0 = ld_unit(rw, WS_T0 + lane * 16, 0);
      const unsigned lead = base - t0[0];
      int d = wide_bad(t0, tag0) == 0 && lead < 64u ? (int)lead : 0;
      d = max(d, dppi<XOR1>(d)); d = max(d, dppi<XOR2>(d)); d = max(d, dppi<ROR4>(d)); d = max(d, dppi<ROR8>(d));
      const int dm = max(max(__builtin_amdgcn_readlane(d, 0), __builtin_amdgcn_readlane(d, 16)),
                         max(__builtin_amdgcn_readlane(d, 32), __builtin_amdgcn_readlane(d, 48)));
      if (lane == 0) lds_st<unsigned>(L, L_FLAG + F_LEAD, (unsigned)dm);
    } else {
      wait_until<TQ>(base);
      have_q = gather_q(TQL);
    }
    NEW_PAGE(2);
    wait_set<2>(L, F_ISS);
    const rsrc_t rv = mk_rsrc(vc + hb, WIN * DH * 2);
    u32x4 V[32], pp[4];
    float acc[16];
#pragma unroll
    for (int v = 0; v < 16; ++v) acc[v] = 0.f;
    const unsigned dt = STEP_V - __builtin_amdgcn_readfirstlane(lds_ld<unsigned>(L, L_FLAG + F_LEAD)) / 2;
    unsigned next = TMID * 16 - dt;
    auto pv = [&](auto uc) {
      constexpr int u = uc;
      if constexpr ((u & 7) == 0) {
#pragma unroll
        for (int j = 0; j < 4; ++j) pp[j] = lds_ld<u32x4>(L, L_PB + wave * 256 + (u >> 3) * 128 + (lane >> 5) * 64 + j * 16);
      }
      pair_rows(V[2 * u], V[2 * u + 1]);
      gemv_pair(V[2 * u], V[2 * u + 1], pp[(u & 7) >> 2][u & 3], acc);
      gemv_pair(V[2 * u], V[2 * u + 1], pp[2 + ((u & 7) >> 2)][u & 3], acc + 8);
      asm volatile("" : "+v"(acc[0]), "+v"(acc[1]), "+v"(acc[2]), "+v"(acc[3]), "+v"(acc[4]), "+v"(acc[5]), "+v"(acc[6]), "+v"(acc[7]),
                        "+v"(acc[8]), "+v"(acc[9]), "+v"(acc[10]), "+v"(acc[11]), "+v"(acc[12]), "+v"(acc[13]), "+v"(acc[14]), "+v"(acc[15]));
    };
    static_for<0, 32>([&](auto ic) {
      constexpr int i = ic;
      FENCE();
      pace(base, next, dt);
      V[i] = ld16<AUX_NT>(rv, kvo[i & 3], (i >> 2) << 17);
      if constexpr (i == 31) signal(L, F_VI, wave, lane);
      FENCE();
      if constexpr (i == 3) { if (!have_q) gather_q(~0u); }
      if constexpr (i == J0) wait_set<1>(L, F_P);
      if constexpr (i == PJ) NEW_PAGE(3);
      static_for<pairs_done(i - 1), pairs_done(i)>(pv);
    });
    const bool mrg = wave == 1 || wave == 2;
    const int hd = wave & 1, pf = L_PART + hd * 1024 + lane * 16;
    int oh1 = WS_H1 + (((r * 8 + (lane >> 3)) * 8 + s) * 16 + hd * 8 + (lane & 7)) * 16;
    u32x4 chkv = chk;
    asm volatile("" : "+v"(oh1), "+v"(chkv));
    float wown = 0.f;
    if (mrg) { wait_set<0>(L, F_W); wown = lds_ld<float>(L, L_ML + 64 + hd * 4); }
    static_for<pairs_done(31), 16>(pv);
    {
      f32x4 lo, hi;
#pragma unroll
      for (int v = 0; v < 4; ++v) { lo[v] = swap32_add(acc[v], acc[v + 8]); hi[v] = swap32_add(acc[v + 4], acc[v + 12]); }
      lds_st<f32x4>(L, L_PART + wave * 2048 + lane * 32, lo);
      lds_st<f32x4>(L, L_PART + wave * 2048 + lane * 32 + 16, hi);
    }
    asm volatile("" ::: "memory");
    signal(L, F_PA, wave, lane);
    if (mrg) {
      f32x4 r0, r1, r2, r3;
#pragma nounroll
      for (unsigned it = 0; it < SPIN_CAP; ++it) {
        asm volatile("" ::: "memory");
        const u32x4 f = lds_ld<u32x4>(L, L_FLAG + F_PA);
        asm volatile("" ::: "memory");
        r0 = lds_ld<f32x4>(L, pf); r1 = lds_ld<f32x4>(L, pf + 2048); r2 = lds_ld<f32x4>(L, pf + 4096); r3 = lds_ld<f32x4>(L, pf + 6144);
        asm volatile("" : "+v"(r0), "+v"(r1), "+v"(r2), "+v"(r3));
        if (__builtin_amdgcn_readfirstlane(f[0] & f[1] & f[2] & f[3])) break;
      }
      st_packed(rw, oh1, wown * ((r0 + r1) + (r2 + r3)), chkv);
    }
    if (wave == 0 && jr < 28) {
      const int oa = WS_H2 + (b * 28 + jr) * 4096 + lane * 16;
      u32x4 a[4];
      wait_set<1>(L, F_H2);
      __builtin_amdgcn_s_sleep(D2);
#pragma nounroll
      for (unsigned it = 0; it < SPIN_CAP; ++it) {
#pragma unroll
        for (int i = 0; i < 4; ++i) a[i] = ld_unit(rw, oa, i * 1024);
        if (__all((strip(a[0], chk) | strip(a[1], chk) | strip(a[2], chk) | strip(a[3], chk)) == 0)) break;
      }
      float v[4];
#pragma unroll
      for (int k = 0; k < 4; ++k) v[k] = (asf(a[0][k]) + asf(a[1][k])) + (asf(a[2][k]) + asf(a[3][k]));
      float y = swap32_add(swap16_add(v[0], v[1]), swap16_add(v[2], v[3]));
      y += dpp<ROR4>(y); y += dpp<ROR8>(y);
      if (!(lane & 12)) *(__attribute__((address_space(1))) unsigned short*)po = (unsigned short)pkbf(y, y);
      if (jr == 0 && lane == 0) __builtin_amdgcn_raw_buffer_store_b32(tag, rw, WS_EPOCH + b * 4, 0, AUX_SC1);
    }
  } else {
    const rsrc_t rk = mk_rsrc(kc + hb, WIN * DH * 2);
    const int rg = lane >> 4, chunk = 16 * w4 + (lane & 15), colb = chunk < 56 ? chunk * 16 : OOB;
    const unsigned short* wo_rows = wo + (size_t)(2 * kvh * DH + 32 * s) * DM;
    const rsrc_t r2 = mk_rsrc(w2 + (size_t)(32 * jf) * DM, ffn ? 32 * DM * 2 : 0);
    const rsrc_t rt = mk_rsrc(w4 == 0 ? vc + hb : wo_rows, w4 < 2 ? WIN * 4 : 0);
    const int v2 = rg * 8 * (DM * 2) + colb;
    u32x4 K[32], W2[8];
    unsigned touch;
    const unsigned dt = prod ? step_k(SP) : step_k(SN);
    unsigned next = (prod ? SP * 16 : SN * 16) - dt;
    static_for<0, 32>([&](auto ic) {
      constexpr int i = ic;
      pace(base, next, dt);
      K[i] = ld16<AUX_NT>(rk, kvo[i & 3], (i >> 2) << 17);
      FENCE();
      if constexpr (i == BTI) { touch = __builtin_amdgcn_raw_buffer_load_b32(rt, lane == 0 ? s * 1024 : OOB, 0, 0); FENCE(); }
      if constexpr (i == 31 - EI) { signal(L, F_ISS, w4, lane); FENCE(); }
      if constexpr ((i & 3) == 3) { W2[i >> 2] = ld16<0>(r2, v2, (((i >> 2) + 2 * b) & 7) * DM * 2); FENCE(); }
    });
    wait_set<2>(L, F_Q);
    asm volatile("" :: "v"(touch), "v"(epoch));
    const u32x4 misc = lds_ld<u32x4>(L, L_MISC);
    const u32x4 chk = check_bits(misc[0]);
    {
      const u32x4 q0 = lds_ld<u32x4>(L, L_QS + (lane & 31) * 16), q1 = lds_ld<u32x4>(L, L_QS + 512 + (lane & 31) * 16);
      const float t0 = scores16(K, q0, q1, lane);
      FENCE();
      const float t1 = scores16(K + 16, q0, q1, lane);
      float m = fmaxf(t0, t1);
      m = fmaxf(m, dpp<XOR2>(m)); m = fmaxf(m, dpp<ROR4>(m)); m = rows_max(fmaxf(m, dpp<ROR8>(m)));
      if (lane < 2) lds_st<float>(L, L_ML + lane * 16 + w4 * 4, m);
      arrive(L, F_M, w4, lane);
      wait_set<0>(L, F_M);
      const f32x4 mw = lds_ld<f32x4>(L, L_ML + (lane & 1) * 16);
      const float M = fmaxf(fmaxf(mw[0], mw[1]), fmaxf(mw[2], mw[3])), ms = M == -INFINITY ? 0.f : M;
      const float p0 = __builtin_amdgcn_exp2f(t0 - ms), p1 = __builtin_amdgcn_exp2f(t1 - ms);
      const float o0 = dpp<XOR2>(p0), o1 = dpp<XOR2>(p1);
      const int pb = L_PB + w4 * 256 + (((lane >> 5) * 2 + (lane & 1)) * 8 + ((lane & 31) >> 2)) * 4;
      if (!(lane & 2)) { lds_st<unsigned>(L, pb, pkbf(p0, o0)); lds_st<unsigned>(L, pb + 128, pkbf(p1, o1)); }
      float l = (p0 + o0) + (p1 + o1);
      l += dpp<ROR4>(l); l += dpp<ROR8>(l); l = swap16_add(l, l); l = swap32_add(l, l);
      if (lane < 2) lds_st<float>(L, L_ML + 32 + lane * 16 + w4 * 4, l);
      arrive(L, F_P, w4, lane);
      if (w4 == 0) {
        wait_set<0>(L, F_P);
        const f32x4 lw = lds_ld<f32x4>(L, L_ML + 32 + (lane & 1) * 16);
        st_wide(rw, lane < 2 ? WS_H1S + (r * 16 + s * 2 + lane) * 16 : OOB, asu(M), asu((lw[0] + lw[1]) + (lw[2] + lw[3])), misc[0]);
        __builtin_amdgcn_s_sleep(DS);
        u32x4 st;
#pragma nounroll
        for (unsigned it = 0; it < SPIN_CAP; ++it) {
          st = ld_unit(rw, WS_H1S + (r * 16 + (lane & 15)) * 16, 0);
          if (__all(wide_bad(st, misc[0]) == 0)) break;
          __builtin_amdgcn_s_sleep(2);
        }
        const float Ms = asf(st[0]);
        float Mg = fmaxf(Ms, dpp<XOR2>(Ms)); Mg = fmaxf(Mg, dpp<ROR4>(Mg)); Mg = fmaxf(Mg, dpp<ROR8>(Mg));
        float Lg = __builtin_amdgcn_exp2f(Ms - Mg) * asf(st[2]);
        Lg += dpp<XOR2>(Lg); Lg += dpp<ROR4>(Lg); Lg += dpp<ROR8>(Lg);
        if (lane < 2) lds_st<float>(L, L_ML + 64 + lane * 4, __builtin_amdgcn_exp2f(M - Mg) / Lg * asf(misc[1]));
        arrive(L, F_W, 0, lane);
      }
    }
    float acc[8];
#pragma unroll
    for (int v = 0; v < 8; ++v) acc[v] = 0.f;
#pragma unroll
    for (int u = 0; u < 4; ++u) {
      pair_rows(W2[2 * u], W2[2 * u + 1]);
      gemv_pair(W2[2 * u], W2[2 * u + 1], lds_ld<unsigned>(L, L_HS + rg * 16 + ((u + b) & 3) * 4), acc);
    }
#pragma unroll
    for (int v = 0; v < 8; ++v) { acc[v] *= asf(misc[2]); asm volatile("" : "+v"(acc[v])); }
    const rsrc_t ro = mk_rsrc(wo_rows, (DH + 32) * DM * 2);
    const int vo = ((rg >> 1) * DH + (rg & 1) * 16) * (DM * 2) + colb;
    const int unit2 = 2 * chunk + (lane >> 5);
    int o2 = (!(lane & 16) && chunk < 56) ? WS_H2 + (((b * 28 + (unit2 >> 2)) * 64 + kvh * 8 + s) * 4 + (unit2 & 3)) * 16 : OOB;
    const int o1 = WS_H1 + ((r * 8 + s) * 128 + ((lane & 15) >> 2) * 16 + 4 * rg + (lane & 3)) * 16;
    u32x4 WO[16];
    asm volatile("" : "+v"(o2));
    NEW_PAGE(1);
    wait_set<1>(L, F_VI);
#pragma unroll
    for (int i = 0; i < 16; ++i) { FENCE(); WO[i] = ld16<AUX_NT>(ro, vo, i * DM * 2); FENCE(); }
    wait_set<1>(L, F_PA);
    __builtin_amdgcn_s_sleep(D1);
    wait_until<TEND + X1>(base - __builtin_amdgcn_readfirstlane(lds_ld<unsigned>(L, L_FLAG + F_LEAD)));
    FENCE();
    u32x4 a0 = ld_unit(rw, o1, 0), a1 = ld_unit(rw, o1, 1024);
    FENCE();
#pragma unroll
    for (int u = 0; u < 8; ++u) { pair_rows(WO[2 * u], WO[2 * u + 1]); asm volatile("" : "+v"(WO[2 * u]), "+v"(WO[2 * u + 1])); }
#pragma nounroll
    for (unsigned it = 0; it < SPIN_CAP; ++it) {
      if (__all((strip(a0, chk) | strip(a1, chk)) == 0)) break;
      a0 = ld_unit(rw, o1, 0); a1 = ld_unit(rw, o1, 1024);
    }
    float v[4];
#pragma unroll
    for (int k = 0; k < 4; ++k) { v[k] = asf(a0[k]) + asf(a1[k]); v[k] += dpp<ROR4>(v[k]); v[k] += dpp<ROR8>(v[k]); }
    const int p0 = pkbf(v[0], v[1]), p1 = pkbf(v[2], v[3]);
    gemv_pair(WO[0], WO[1], dppi<0x00>(p0), acc); gemv_pair(WO[2], WO[3], dppi<0x00>(p1), acc);
    gemv_pair(WO[4], WO[5], dppi<0x55>(p0), acc); gemv_pair(WO[6], WO[7], dppi<0x55>(p1), acc);
    gemv_pair(WO[8], WO[9], dppi<0xAA>(p0), acc); gemv_pair(WO[10], WO[11], dppi<0xAA>(p1), acc);
    gemv_pair(WO[12], WO[13], dppi<0xFF>(p0), acc); gemv_pair(WO[14], WO[15], dppi<0xFF>(p1), acc);
    f32x4 z;
#pragma unroll
    for (int i = 0; i < 4; ++i) { z[i] = swap32_add(acc[i], acc[i + 4]); z[i] = swap16_add(z[i], z[i]); }
    st_packed(rw, o2, z, chk);
    signal(L, F_H2, w4, lane);
  }
#endif
}

extern "C" void kernel_launch(void* const* d_in, const int* in_sizes, int n_in, void* d_out, int out_size, void* d_ws,
                              size_t ws_size, hipStream_t stream) {
  typedef const unsigned short* P;
  fused_decode<<<256, 512, 0, stream>>>((P)d_in[0], (P)d_in[1], (P)d_in[2], (P)d_in[3], (P)d_in[4], (P)d_in[6], (P)d_in[7],
                                        (P)d_in[8], (unsigned short*)d_out, (char*)d_ws);
}
```
